```python
import torch

def _softmax(x, axis=-1):
    return torch.softmax(x, dim=axis)
def _gelu(x):
    return torch.nn.functional.gelu(x, approximate="tanh")
def _silu(x):
    return torch.nn.functional.silu(x)
def _sigmoid(x):
    return torch.sigmoid(x)
def _layernorm(x, axis=-1, eps=1e-5):
    m = x.mean(dim=axis, keepdim=True)
    v = x.var(dim=axis, keepdim=True, unbiased=False)
    return (x - m) / torch.sqrt(v + eps)
def _scatter_add(tgt, idx, src, axis=0):
    return tgt.clone().index_add_(axis, idx, src)
def _segment_sum(src, seg, n):
    out = torch.zeros((n,) + src.shape[1:], dtype=src.dtype)
    return out.index_add_(0, seg, src)
def _pad(x, pw):
    flat = tuple(v for pair in reversed(pw) for v in pair)
    return torch.nn.functional.pad(x, flat)

kernel_name = 'gated_mix__sliding_block_cached__gqa_block_cached__decode'

def setup_inputs():

    _w = (lambda a: (lambda t: t.to(torch.bfloat16) if t.is_floating_point() else t)(torch.as_tensor(a)))
    def _A_setup():
        B, Sq, D = 2, 1, 1024
        Hq, Hkv, Sk, Dh = 32, 4, 32768, 128
        assert Hq % Hkv == 0, (Hq, Hkv)
        return {
            "x": _w((lambda *s: torch.randn(*s, dtype=torch.bfloat16) * 0.05)(B, Sq, D)),
            "k_cache": _w((lambda *s: torch.randn(*s, dtype=torch.bfloat16) * 6.4)(B, Hkv, Sk, Dh)),
            "v_cache": _w((lambda *s: torch.randn(*s, dtype=torch.bfloat16) * 0.05)(B, Hkv, Sk, Dh)),
            "wq": _w((lambda *s: torch.randn(*s, dtype=torch.bfloat16) * 6.4)(D, Hq * Dh) * (D ** -0.5)),
            "wo": _w((lambda *s: torch.randn(*s, dtype=torch.bfloat16) * 0.05)(Hq * Dh, D) * ((Hq * Dh) ** -0.5)),
        }
    _A_in = _A_setup()

    def _B_setup():
        B, Sq, D = 2, 1, 1024
        Hq, Hkv, Sk, Dh = 32, 4, 32768, 128
        assert Hq % Hkv == 0, (Hq, Hkv)
        return {
            "x": _w((lambda *s: torch.randn(*s, dtype=torch.bfloat16) * 0.05)(B, Sq, D)),
            "k_cache": _w((lambda *s: torch.randn(*s, dtype=torch.bfloat16) * 6.4)(B, Hkv, Sk, Dh)),
            "v_cache": _w((lambda *s: torch.randn(*s, dtype=torch.bfloat16) * 0.05)(B, Hkv, Sk, Dh)),
            "wq": _w((lambda *s: torch.randn(*s, dtype=torch.bfloat16) * 6.4)(D, Hq * Dh) * (D ** -0.5)),
            "wo": _w((lambda *s: torch.randn(*s, dtype=torch.bfloat16) * 0.05)(Hq * Dh, D) * ((Hq * Dh) ** -0.5)),
        }
    _B_in = _B_setup()

    return {
        "A_x": _A_in["x"],
        "A_k_cache": _A_in["k_cache"],
        "A_v_cache": _A_in["v_cache"],
        "A_wq": _A_in["wq"],
        "A_wo": _A_in["wo"],
        "B_x": _B_in["x"],
        "B_k_cache": _B_in["k_cache"],
        "B_v_cache": _B_in["v_cache"],
        "B_wq": _B_in["wq"],
        "B_wo": _B_in["wo"],
        "gate_w": _w((lambda *s: torch.randn(*s, dtype=torch.bfloat16) * 0.05)(1024) * 16.0),
    }

def reference(A_x, A_k_cache, A_v_cache, A_wq, A_wo, B_x, B_k_cache, B_v_cache, B_wq, B_wo, gate_w):
    def _A_ref(x, k_cache, v_cache, wq, wo):
        B, Sq, D = x.shape
        Hq, Dh = 32, 128
        Hkv, Sk = k_cache.shape[1], k_cache.shape[2]
        W = min(4096, max(256, Sk // 4))
        rep = Hq // Hkv
        q = (lambda x, *d: x.permute(*d))((x @ wq).reshape(B, Sq, Hq, Dh), 0, 2, 1, 3)
        k = (lambda x, n, axis: torch.repeat_interleave(x, n, dim=axis))(k_cache[:, :, Sk - W:, :], rep, axis=1)
        v = (lambda x, n, axis: torch.repeat_interleave(x, n, dim=axis))(v_cache[:, :, Sk - W:, :], rep, axis=1)
        scores = torch.einsum("bhqd,bhkd->bhqk", q, k) * (Dh ** -0.5)
        p = _softmax(scores, axis=-1)
        o = torch.einsum("bhqk,bhkd->bhqd", p, v)
        return (lambda x, *d: x.permute(*d))(o, 0, 2, 1, 3).reshape(B, Sq, Hq * Dh) @ wo

    def _B_ref(x, k_cache, v_cache, wq, wo):
        B, Sq, D = x.shape
        Hq, Dh = 32, 128
        Hkv = k_cache.shape[1]
        rep = Hq // Hkv
        q = (lambda x, *d: x.permute(*d))((x @ wq).reshape(B, Sq, Hq, Dh), 0, 2, 1, 3)
        k = (lambda x, n, axis: torch.repeat_interleave(x, n, dim=axis))(k_cache, rep, axis=1)
        v = (lambda x, n, axis: torch.repeat_interleave(x, n, dim=axis))(v_cache, rep, axis=1)
        scores = torch.einsum("bhqd,bhkd->bhqk", q, k) * (Dh ** -0.5)
        p = _softmax(scores, axis=-1)
        o = torch.einsum("bhqk,bhkd->bhqd", p, v)
        return (lambda x, *d: x.permute(*d))(o, 0, 2, 1, 3).reshape(B, Sq, Hq * Dh) @ wo

    x = A_x
    g = _sigmoid((x * gate_w).sum(-1, keepdims=True))
    a = _A_ref(x, A_k_cache, A_v_cache, A_wq, A_wo)
    b = _B_ref(x, B_k_cache, B_v_cache, B_wq, B_wo)
    return g * a + (1 - g) * b
```

```cpp
#include <hip/hip_runtime.h>
#include <hip/hip_bf16.h>
#include <stdint.h>

#define GAS __attribute__((address_space(1)))
#define LAS __attribute__((address_space(3)))
typedef unsigned v4u __attribute__((ext_vector_type(4)));
typedef unsigned v2u __attribute__((ext_vector_type(2)));
typedef float f32x4 __attribute__((ext_vector_type(4)));
typedef short bf16x8 __attribute__((ext_vector_type(8)));
typedef __bf16 bf16x2_t __attribute__((ext_vector_type(2)));
typedef unsigned long long u64;

namespace {

constexpr int DM = 1024, HKV = 4, SK = 32768, DH = 128, WIN = 4096, NQ = 4096;
constexpr int NWG = 256, NTHR = 512;
constexpr float C2 = 0.08838834764831845f * 1.4426950408889634f;

constexpr int WS_EPOCH = 0;
constexpr int WS_FLAGS = 256;
constexpr int WS_QG = 4096;
constexpr int WS_ML = WS_QG + 2 * 2 * NQ * 8;
constexpr int WS_H2 = WS_ML + 64 * 64 * 8;
constexpr int WS_H3 = WS_H2 + NWG * 8192;
constexpr int WS_END = WS_H3 + NWG * 8192;

constexpr int AUX_NT = 2, AUX_SC1 = 16;
constexpr unsigned SENT = 0xFFFFFFFFu;
constexpr unsigned SPIN_CAP = 1u << 17;

constexpr int PSTR = 132;
constexpr int L_PART = 0;
constexpr int L_ML = L_PART + 2 * 8 * 8 * PSTR * 4;
constexpr int L_Q = L_ML + 2 * 8 * 8 * 8;
constexpr int L_QP = L_Q + 2 * 8 * 136 * 2;
constexpr int L_GP = L_QP + 8 * 64 * 4;
constexpr int L_CB = L_GP + 64;
constexpr int L_FN = L_CB + 8 * 32 * 4;
constexpr int L_END = L_FN + 8 * 8 * 4;

__device__ __forceinline__ f32x4 mfma(v4u a, v4u b, f32x4 c) {
  return __builtin_amdgcn_mfma_f32_16x16x32_bf16(__builtin_bit_cast(bf16x8, a), __builtin_bit_cast(bf16x8, b), c, 0, 0, 0);
}
__device__ __forceinline__ float dot2(unsigned a, unsigned b, float c) {
  return __builtin_amdgcn_fdot2_f32_bf16(__builtin_bit_cast(bf16x2_t, a), __builtin_bit_cast(bf16x2_t, b), c, false);
}
__device__ __forceinline__ unsigned pk_bf16(float lo, float hi) {
  bf16x2_t r; r.x = (__bf16)lo; r.y = (__bf16)hi;
  return __builtin_bit_cast(unsigned, r);
}
__device__ __forceinline__ unsigned perm_lo(unsigned hi_tok, unsigned lo_tok) { return __builtin_amdgcn_perm(hi_tok, lo_tok, 0x05040100u); }
__device__ __forceinline__ unsigned perm_hi(unsigned hi_tok, unsigned lo_tok) { return __builtin_amdgcn_perm(hi_tok, lo_tok, 0x07060302u); }
template <int CTRL> __device__ __forceinline__ float dpp(float x) {
  return __builtin_bit_cast(float, __builtin_amdgcn_mov_dpp(__builtin_bit_cast(int, x), CTRL, 0xf, 0xf, true));
}
constexpr int ROR4 = 0x124, ROR8 = 0x128, XOR1 = 0xB1, XOR2 = 0x4E;
__device__ __forceinline__ float swap32_add(float a, float b) {
  auto r = __builtin_amdgcn_permlane32_swap(__float_as_uint(a), __float_as_uint(b), false, false);
  return __uint_as_float(r[0]) + __uint_as_float(r[1]);
}
__device__ __forceinline__ float swap16_add(float a, float b) {
  auto r = __builtin_amdgcn_permlane16_swap(__float_as_uint(a), __float_as_uint(b), false, false);
  return __uint_as_float(r[0]) + __uint_as_float(r[1]);
}
__device__ __forceinline__ float xrow_max(float x) {
  auto s = __builtin_amdgcn_permlane16_swap(__float_as_uint(x), __float_as_uint(x), false, false);
  x = fmaxf(__uint_as_float(s[0]), __uint_as_float(s[1]));
  auto t = __builtin_amdgcn_permlane32_swap(__float_as_uint(x), __float_as_uint(x), false, false);
  return fmaxf(__uint_as_float(t[0]), __uint_as_float(t[1]));
}
__device__ __forceinline__ float xrow_sum(float x) { x = swap16_add(x, x); return swap32_add(x, x); }
__device__ __forceinline__ float ex2(float x) { return __builtin_amdgcn_exp2f(x); }

#define LDS_BARRIER() asm volatile("s_waitcnt lgkmcnt(0)\n\ts_barrier" ::: "memory")
#define RAW_BARRIER() asm volatile("s_barrier" ::: "memory")
#define PIN() __builtin_amdgcn_sched_barrier(0)

__device__ __forceinline__ int opq(int v) { asm volatile("" : "+v"(v)); return v; }

typedef __amdgpu_buffer_rsrc_t rsrc_t;
__device__ __forceinline__ rsrc_t mk(const void* p, int bytes) { return __builtin_amdgcn_make_buffer_rsrc((void*)p, 0, bytes, 0x00020000); }
__device__ __forceinline__ v4u ldnt(rsrc_t r, int voff, int soff) { return __builtin_amdgcn_raw_buffer_load_b128(r, voff, soff, AUX_NT); }

template <int NT> __device__ __forceinline__ void issue_k(v4u (&K)[4 * NT], rsrc_t r, int voff, int soff) {
  voff = opq(voff);
#pragma unroll
  for (int i = 0; i < 4 * NT; ++i) K[i] = ldnt(r, voff + (i & 3) * 64, soff + (i >> 2) * 4096);
  PIN();
}
template <int NT> __device__ __forceinline__ void issue_v(v4u (&V)[4 * NT], rsrc_t r, int voff, int soff) {
  voff = opq(voff);
#pragma unroll
  for (int i = 0; i < 4 * NT; ++i) V[i] = ldnt(r, voff + (i & 3) * 256, soff + (i >> 2) * 4096);
  PIN();
}

template <int NT, bool FIRST>
__device__ __forceinline__ v4u qk_softmax(const v4u (&K)[4 * NT], const v4u (&qf)[4], float& m, float& l, f32x4 (&O)[8]) {
  f32x4 s[NT];
#pragma unroll
  for (int t = 0; t < NT; ++t) {
    s[t] = f32x4{0.f, 0.f, 0.f, 0.f};
#pragma unroll
    for (int kk = 0; kk < 4; ++kk) s[t] = mfma(K[4 * t + kk], qf[kk], s[t]);
  }
  float mx = fmaxf(fmaxf(s[0][0], s[0][1]), fmaxf(s[0][2], s[0][3]));
  if (NT == 2) mx = fmaxf(mx, fmaxf(fmaxf(s[NT - 1][0], s[NT - 1][1]), fmaxf(s[NT - 1][2], s[NT - 1][3])));
  mx = xrow_max(mx);
  const float mn = FIRST ? mx : fmaxf(m, mx);
  const float mc = (mn == -INFINITY) ? 0.f : mn * C2;
  float p[4 * NT], sum = 0.f;
#pragma unroll
  for (int t = 0; t < NT; ++t)
#pragma unroll
    for (int i = 0; i < 4; ++i) { p[4 * t + i] = ex2(fmaf(s[t][i], C2, -mc)); sum += p[4 * t + i]; }
  if (FIRST) l = sum;
  else {
    const float alpha = ex2(fmaf(m, C2, -mc));
    l = fmaf(l, alpha, sum);
#pragma unroll
    for (int e = 0; e < 8; ++e) O[e] *= alpha;
  }
  m = mn;
  v4u P;
  P.x = pk_bf16(p[0], p[1]); P.y = pk_bf16(p[2], p[3]);
  if (NT == 2) { P.z = pk_bf16(p[4 * NT - 4], p[4 * NT - 3]); P.w = pk_bf16(p[4 * NT - 2], p[4 * NT - 1]); }
  else { P.z = 0u; P.w = 0u; }
  return P;
}
template <int NT, bool FIRST>
__device__ __forceinline__ void pv(const v4u (&V)[4 * NT], v4u P, f32x4 (&O)[8]) {
#pragma unroll
  for (int e = 0; e < 8; ++e) {
    const int wd = e >> 1;
    v4u f;
    if (e & 1) {
      f.x = perm_hi(V[1][wd], V[0][wd]); f.y = perm_hi(V[3][wd], V[2][wd]);
      if (NT == 2) { f.z = perm_hi(V[4 * NT - 3][wd], V[4 * NT - 4][wd]); f.w = perm_hi(V[4 * NT - 1][wd], V[4 * NT - 2][wd]); }
      else { f.z = 0u; f.w = 0u; }
    } else {
      f.x = perm_lo(V[1][wd], V[0][wd]); f.y = perm_lo(V[3][wd], V[2][wd]);
      if (NT == 2) { f.z = perm_lo(V[4 * NT - 3][wd], V[4 * NT - 4][wd]); f.w = perm_lo(V[4 * NT - 1][wd], V[4 * NT - 2][wd]); }
      else { f.z = 0u; f.w = 0u; }
    }
    O[e] = mfma(f, P, FIRST ? f32x4{0.f, 0.f, 0.f, 0.f} : O[e]);
  }
}
__device__ __forceinline__ void dump_partial(LAS unsigned char* L, int br, int w, int g, int cc, float m, float l, const f32x4 (&O)[8]) {
  l = xrow_sum(l);
  if (cc < 8) {
    LAS float* rec = (LAS float*)(L + L_PART) + ((br * 8 + w) * 8 + cc) * PSTR + 32 * g;
#pragma unroll
    for (int i = 0; i < 4; ++i) {
      *(LAS f32x4*)(rec + 8 * i) = f32x4{O[0][i], O[1][i], O[2][i], O[3][i]};
      *(LAS f32x4*)(rec + 8 * i + 4) = f32x4{O[4][i], O[5][i], O[6][i], O[7][i]};
    }
    if (g == 0) {
      LAS float* ml = (LAS float*)(L + L_ML) + ((br * 8 + w) * 8 + cc) * 2;
      ml[0] = m * C2; ml[1] = l;
    }
  }
}
__device__ __forceinline__ void load_qf(LAS unsigned char* L, int br, int g, int cc, v4u (&qf)[4]) {
  LAS unsigned char* q = L + L_Q + ((br * 8 + (cc & 7)) * 136 + 8 * g) * 2;
#pragma unroll
  for (int kk = 0; kk < 4; ++kk) qf[kk] = *(LAS v4u*)(q + kk * 64);
}
__device__ __forceinline__ unsigned canon(float v) { const unsigned u = __float_as_uint(v); return u < 0xFFFFFFFEu ? u : 0xFFFFFFFEu; }
__device__ __forceinline__ bool has_sent(v4u v) {
  const unsigned a = v.x > v.y ? v.x : v.y, b = v.z > v.w ? v.z : v.w;
  return (a > b ? a : b) == SENT;
}
__device__ __forceinline__ u64 mix64(u64 z) {
  z = (z ^ (z >> 30)) * 0xBF58476D1CE4E5B9ull;
  z = (z ^ (z >> 27)) * 0x94D049BB133111EBull;
  return z ^ (z >> 31);
}

#define STAMP(i) do { } while (0)

__global__ void __launch_bounds__(NTHR, 2)
gated_mix_attn_decode_kernel(const unsigned short* __restrict__ x, const unsigned short* __restrict__ kA, const unsigned short* __restrict__ vA,
                             const unsigned short* __restrict__ wqA, const unsigned short* __restrict__ woA,
                             const unsigned short* __restrict__ kB, const unsigned short* __restrict__ vB,
                             const unsigned short* __restrict__ wqB, const unsigned short* __restrict__ woB,
                             const unsigned short* __restrict__ gate_w, unsigned short* __restrict__ out, unsigned char* ws) {
  __shared__ __attribute__((aligned(16))) unsigned char smem[L_END];
  LAS unsigned char* L = (LAS unsigned char*)smem;
  const int tid = threadIdx.x, lane = tid & 63, w = __builtin_amdgcn_readfirstlane(tid >> 6), c = blockIdx.x;
  const int g = lane >> 4, cc = lane & 15;
  STAMP(0);
  const int b = c >> 7, kvh = (c >> 5) & 3, sp = c & 31;
  const int slice = ((((c >> 4) << 3) + (c & 7)) << 1) + ((c >> 3) & 1);
  const int brq = slice >> 7, col0 = (slice & 127) * 32;
  const int bro = c >> 7, orow0 = (c & 127) * 32;

  const rsrc_t rws = mk(ws, WS_END);
  const rsrc_t rwq = mk(brq ? wqB : wqA, DM * NQ * 2);
  const rsrc_t rwo = mk(bro ? woB : woA, NQ * DM * 2);
  const rsrc_t rkA = mk(kA, 2 * HKV * SK * DH * 2), rvA = mk(vA, 2 * HKV * SK * DH * 2);
  const rsrc_t rkB = mk(kB, 2 * HKV * SK * DH * 2), rvB = mk(vB, 2 * HKV * SK * DH * 2);

  const v2u epoch_v = __builtin_amdgcn_raw_buffer_load_b64(rws, opq(WS_EPOCH), 0, AUX_SC1);

  auto h2off = [&](int t) { return WS_H2 + ((t >> 8) * 128 + (kvh * 8 + ((t >> 5) & 7)) * 4 + ((t & 31) >> 3)) * 8192 + (b * 32 + sp) * 128 + (t & 7) * 16; };
  auto mloff = [&](int t) { return WS_ML + ((((t >> 8) * 4 + kvh) * 8 + ((t >> 5) & 7)) * 64 + b * 32 + sp) * 8; };
  {
    const v4u ones = {SENT, SENT, SENT, SENT};
    __builtin_amdgcn_raw_buffer_store_b128(ones, rws, h2off(tid), 0, AUX_SC1);
    __builtin_amdgcn_raw_buffer_store_b128(ones, rws, WS_H3 + c * 8192 + tid * 16, 0, AUX_SC1);
    if ((tid & 31) == 0) __builtin_amdgcn_raw_buffer_store_b64(v2u{SENT, SENT}, rws, mloff(tid), 0, AUX_SC1);
  }
  PIN();
  const int rr = lane >> 2, cq = lane & 3;
  const v4u x0 = *(const GAS v4u*)(x + 128 * w + 8 * rr);
  const v4u x1 = *(const GAS v4u*)(x + DM + 128 * w + 8 * rr);
  const v4u gw = *(const GAS v4u*)(gate_w + 128 * w + 8 * rr);
  v4u Wq[8];
  {
    const int voff = (128 * w + 8 * rr) * (NQ * 2) + col0 * 2 + cq * 16;
#pragma unroll
    for (int j = 0; j < 8; ++j) Wq[j] = ldnt(rwq, voff, j * (NQ * 2));
  }
  PIN();
  RAW_BARRIER();
  const int kvo = g * 16 + cc * 256;
  const int vvo = g * 4 * 256 + cc * 16;
  const int baseA = (((b * HKV + kvh) * SK) + (SK - WIN) + sp * 128 + w * 16) * (DH * 2);
  const int baseB = (((b * HKV + kvh) * SK) + sp * 1024 + w * 32) * (DH * 2);
  constexpr int USTR = 256 * DH * 2;
  v4u KA[4], VA[4], K0[8], V0[8], K1[8], V1[8];
  issue_k<1>(KA, rkA, kvo, baseA);
  issue_v<1>(VA, rvA, vvo, baseA);
  issue_k<2>(K0, rkB, kvo, baseB);
  STAMP(1);
  {
    float a0[8], a1[8];
#pragma unroll
    for (int e = 0; e < 8; ++e) { a0[e] = 0.f; a1[e] = 0.f; }
#pragma unroll
    for (int jp = 0; jp < 4; ++jp) {
#pragma unroll
      for (int d = 0; d < 4; ++d) {
        const unsigned lo = perm_lo(Wq[2 * jp + 1][d], Wq[2 * jp][d]), hi = perm_hi(Wq[2 * jp + 1][d], Wq[2 * jp][d]);
        a0[2 * d] = dot2(lo, x0[jp], a0[2 * d]); a0[2 * d + 1] = dot2(hi, x0[jp], a0[2 * d + 1]);
        a1[2 * d] = dot2(lo, x1[jp], a1[2 * d]); a1[2 * d + 1] = dot2(hi, x1[jp], a1[2 * d + 1]);
      }
    }
    float g0 = 0.f, g1 = 0.f;
#pragma unroll
    for (int jp = 0; jp < 4; ++jp) { g0 = dot2(gw[jp], x0[jp], g0); g1 = dot2(gw[jp], x1[jp], g1); }
    float r1[8], r2[4];
#pragma unroll
    for (int e = 0; e < 8; ++e) r1[e] = swap32_add(a0[e], a1[e]);
#pragma unroll
    for (int e = 0; e < 4; ++e) r2[e] = swap16_add(r1[e], r1[e + 4]);
#pragma unroll
    for (int e = 0; e < 4; ++e) { r2[e] += dpp<ROR8>(r2[e]); r2[e] += dpp<ROR4>(r2[e]); }
    float gs = swap32_add(g0, g1);
    gs = swap16_add(gs, gs);
    gs += dpp<ROR8>(gs); gs += dpp<ROR4>(gs);
    if ((cc >> 2) == 0)
      *(LAS f32x4*)(L + L_QP + (w * 64 + (g >> 1) * 32 + 8 * cq + 4 * (g & 1)) * 4) = f32x4{r2[0], r2[1], r2[2], r2[3]};
    if ((lane & 31) == 0) *(LAS float*)(L + L_GP + (w * 2 + (lane >> 5)) * 4) = gs;
  }
  STAMP(2);
  LDS_BARRIER();
  const u64 epoch = ((u64)(unsigned)__builtin_amdgcn_readfirstlane(epoch_v.y) << 32) | (unsigned)__builtin_amdgcn_readfirstlane(epoch_v.x);
  const u64 tag = mix64(epoch + 0x9E3779B97F4A7C15ull);
  if (w == 0) {
    float q = 0.f;
#pragma unroll
    for (int ww = 0; ww < 8; ++ww) q += *(LAS float*)(L + L_QP + (ww * 64 + lane) * 4);
    const unsigned qb = pk_bf16(q, 0.f) & 0xFFFFu;
    const u64 gr = (tag & ~0xFFFFull) | qb;
    __hip_atomic_store((GAS u64*)(ws + WS_QG) + ((brq * 2 + (lane >> 5)) * NQ + col0 + (lane & 31)), gr, __ATOMIC_RELAXED, __HIP_MEMORY_SCOPE_AGENT);
    if (lane == 0) __hip_atomic_store((GAS u64*)(ws + WS_FLAGS) + c, tag, __ATOMIC_RELAXED, __HIP_MEMORY_SCOPE_AGENT);
  }
  PIN();
  STAMP(3);
  issue_v<2>(V0, rvB, vvo, baseB);
  issue_k<2>(K1, rkB, kvo, baseB + USTR);
  const unsigned thi = (unsigned)(tag >> 32), tlo = (unsigned)tag;
  const int qoff = WS_QG + (((tid >> 8) * 2 + b) * NQ + kvh * 1024 + 4 * (tid & 255)) * 8;
  const int foff = WS_FLAGS + (tid & 255) * 8;
  v4u ga = __builtin_amdgcn_raw_buffer_load_b128(rws, qoff, 0, AUX_SC1);
  v4u gb = __builtin_amdgcn_raw_buffer_load_b128(rws, qoff + 16, 0, AUX_SC1);
  v2u fl = __builtin_amdgcn_raw_buffer_load_b64(rws, foff, 0, AUX_SC1);
  PIN();
  issue_v<2>(V1, rvB, vvo, baseB + USTR);
  STAMP(4);
  {
    unsigned spins = 0;
    for (;;) {
      const bool ok = ga.y == thi && ga.w == thi && gb.y == thi && gb.w == thi && ((ga.x ^ tlo) >> 16) == 0 && ((ga.z ^ tlo) >> 16) == 0 &&
                      ((gb.x ^ tlo) >> 16) == 0 && ((gb.z ^ tlo) >> 16) == 0 && fl.y == thi && fl.x == tlo;
      if (__all(ok) || ++spins > SPIN_CAP) break;
      __builtin_amdgcn_s_sleep(2);
      asm volatile("" ::: "memory");
      const int qo = opq(qoff);
      ga = __builtin_amdgcn_raw_buffer_load_b128(rws, qo, 0, AUX_SC1);
      gb = __builtin_amdgcn_raw_buffer_load_b128(rws, qo + 16, 0, AUX_SC1);
      fl = __builtin_amdgcn_raw_buffer_load_b64(rws, opq(foff), 0, AUX_SC1);
    }
    v2u qv; qv.x = (ga.x & 0xFFFFu) | (ga.z << 16); qv.y = (gb.x & 0xFFFFu) | (gb.z << 16);
    *(LAS v2u*)(L + L_Q + (((tid >> 8) * 8 + ((tid >> 5) & 7)) * 136 + 4 * (tid & 31)) * 2) = qv;
  }
  STAMP(5);
  LDS_BARRIER();
  if (c == 0 && tid == 0) __hip_atomic_store((GAS u64*)(ws + WS_EPOCH), epoch + 0x9E3779B97F4A7C15ull, __ATOMIC_RELAXED, __HIP_MEMORY_SCOPE_AGENT);
  STAMP(6);

  float m, l;
  f32x4 O[8];
  v4u qf[4];
  load_qf(L, 0, g, cc, qf);
  {
    const v4u P = qk_softmax<1, true>(KA, qf, m, l, O);
    pv<1, true>(VA, P, O);
    dump_partial(L, 0, w, g, cc, m, l, O);
  }
  PIN();
  v4u K2[8], V2[8], K3[8], V3[8], Wo[8];
  issue_k<2>(K2, rkB, kvo, baseB + 2 * USTR);
  STAMP(7);
  load_qf(L, 1, g, cc, qf);
  {
    const v4u P = qk_softmax<2, true>(K0, qf, m, l, O);
    PIN();
    issue_v<2>(V2, rvB, vvo, baseB + 2 * USTR);
    pv<2, true>(V0, P, O);
    PIN();
    issue_k<2>(K3, rkB, kvo, baseB + 3 * USTR);
  }
  {
    const v4u P = qk_softmax<2, false>(K1, qf, m, l, O);
    PIN();
    {
      const int voff = (orow0 + 8 * g) * (DM * 2) + w * 256 + cc * 16;
#pragma unroll
      for (int j = 0; j < 8; ++j) Wo[j] = ldnt(rwo, voff, j * (DM * 2));
      PIN();
    }
    pv<2, false>(V1, P, O);
    PIN();
    issue_v<2>(V3, rvB, vvo, baseB + 3 * USTR);
  }
  STAMP(8);
  {
    const v4u P = qk_softmax<2, false>(K2, qf, m, l, O);
    pv<2, false>(V2, P, O);
  }
  STAMP(9);
  v4u Wf[8];
  {
    const v4u P = qk_softmax<2, false>(K3, qf, m, l, O);
#pragma unroll
    for (int e = 0; e < 8; ++e) {
      const int wd = e >> 1;
      if (e & 1) Wf[e] = v4u{perm_hi(Wo[1][wd], Wo[0][wd]), perm_hi(Wo[3][wd], Wo[2][wd]), perm_hi(Wo[5][wd], Wo[4][wd]), perm_hi(Wo[7][wd], Wo[6][wd])};
      else Wf[e] = v4u{perm_lo(Wo[1][wd], Wo[0][wd]), perm_lo(Wo[3][wd], Wo[2][wd]), perm_lo(Wo[5][wd], Wo[4][wd]), perm_lo(Wo[7][wd], Wo[6][wd])};
    }
    pv<2, false>(V3, P, O);
    dump_partial(L, 1, w, g, cc, m, l, O);
  }
  STAMP(10);
  LDS_BARRIER();
  {
    const int t2 = opq(tid), mbr = t2 >> 8, mh = (t2 >> 5) & 7, mpart = t2 & 31;
    LAS float* ml = (LAS float*)(L + L_ML) + (mbr * 64 + mh) * 2;
    LAS float* rec = (LAS float*)(L + L_PART) + (mbr * 64 + mh) * PSTR + 4 * mpart;
    float mw[8], lw[8];
#pragma unroll
    for (int ww = 0; ww < 8; ++ww) { const v2u t = *(LAS v2u*)(ml + ww * 16); mw[ww] = __uint_as_float(t.x); lw[ww] = __uint_as_float(t.y); }
    float M = mw[0];
#pragma unroll
    for (int ww = 1; ww < 8; ++ww) M = fmaxf(M, mw[ww]);
    const float Ms = (M == -INFINITY) ? 0.f : M;
    float Ls = 0.f;
    f32x4 acc = {0.f, 0.f, 0.f, 0.f};
#pragma unroll
    for (int ww = 0; ww < 8; ++ww) {
      const float f = ex2(mw[ww] - Ms);
      Ls = fmaf(f, lw[ww], Ls);
      acc += f * *(LAS f32x4*)(rec + ww * 8 * PSTR);
    }
    __builtin_amdgcn_raw_buffer_store_b128(v4u{canon(acc[0]), canon(acc[1]), canon(acc[2]), canon(acc[3])}, rws, h2off(t2), 0, AUX_SC1);
    if (mpart == 0) __builtin_amdgcn_raw_buffer_store_b64(v2u{canon(M), canon(Ls)}, rws, mloff(t2), 0, AUX_SC1);
  }
  STAMP(11);
  {
    const int poff = WS_H2 + c * 8192 + tid * 16;
    const int mlb = WS_ML + ((c >> 2) * 64 + (w >> 2) * 32) * 8;
    const int mlall = mlb + (lane & 31) * 8, mlown = mlb + (8 * (w & 3) + (lane >> 3)) * 8;
    v4u pay = __builtin_amdgcn_raw_buffer_load_b128(rws, poff, 0, AUX_SC1);
    v2u ma = __builtin_amdgcn_raw_buffer_load_b64(rws, mlall, 0, AUX_SC1);
    v2u mo = __builtin_amdgcn_raw_buffer_load_b64(rws, mlown, 0, AUX_SC1);
    unsigned spins = 0;
    for (;;) {
      const bool bad = has_sent(pay) || ma.x == SENT || ma.y == SENT || mo.x == SENT || mo.y == SENT;
      if (!__any(bad) || ++spins > SPIN_CAP) break;
      __builtin_amdgcn_s_sleep(1);
      asm volatile("" ::: "memory");
      pay = __builtin_amdgcn_raw_buffer_load_b128(rws, opq(poff), 0, AUX_SC1);
      ma = __builtin_amdgcn_raw_buffer_load_b64(rws, opq(mlall), 0, AUX_SC1);
      mo = __builtin_amdgcn_raw_buffer_load_b64(rws, opq(mlown), 0, AUX_SC1);
    }
    const float Ma = __uint_as_float(ma.x), La = __uint_as_float(ma.y);
    float M = Ma;
    M = fmaxf(M, dpp<XOR1>(M)); M = fmaxf(M, dpp<XOR2>(M)); M = fmaxf(M, dpp<ROR4>(M)); M = fmaxf(M, dpp<ROR8>(M));
    M = xrow_max(M);
    const float Ms = (M == -INFINITY) ? 0.f : M;
    float T = La * ex2(Ma - Ms);
    T += dpp<XOR1>(T); T += dpp<XOR2>(T); T += dpp<ROR4>(T); T += dpp<ROR8>(T);
    T = swap16_add(T, T);
    const float f = ex2(__uint_as_float(mo.x) - Ms) / T;
    float v[4];
#pragma unroll
    for (int i = 0; i < 4; ++i) {
      v[i] = f * __uint_as_float(pay[i]);
      v[i] += dpp<ROR8>(v[i]);
      v[i] = xrow_sum(v[i]);
    }
    if (lane < 8) *(LAS f32x4*)(L + L_CB + (w * 32 + 4 * lane) * 4) = f32x4{v[0], v[1], v[2], v[3]};
  }
  STAMP(12);
  LDS_BARRIER();
  {
    const int ob = (cc >> 2) & 1;
    LAS float* cb = (LAS float*)(L + L_CB) + ob * 128 + 8 * g;
    f32x4 o0 = {0.f, 0.f, 0.f, 0.f}, o1 = {0.f, 0.f, 0.f, 0.f};
#pragma unroll
    for (int ww = 0; ww < 4; ++ww) { o0 += *(LAS f32x4*)(cb + ww * 32); o1 += *(LAS f32x4*)(cb + ww * 32 + 4); }
    const v4u of = {pk_bf16(o0[0], o0[1]), pk_bf16(o0[2], o0[3]), pk_bf16(o1[0], o1[1]), pk_bf16(o1[2], o1[3])};
    float z = 0.f;
#pragma unroll
    for (int ww = 0; ww < 8; ++ww) z += *(LAS float*)(L + L_GP + (ww * 2 + (g & 1)) * 4);
    const float gate = 1.f / (1.f + __expf(-z));
    const float gf = bro ? 1.f - gate : gate;
    float y[8];
#pragma unroll
    for (int e = 0; e < 8; ++e) y[e] = mfma(of, Wf[e], f32x4{0.f, 0.f, 0.f, 0.f})[0];
    const unsigned him = g >= 2 ? ~0u : 0u;
    v4u o;
#pragma unroll
    for (int t = 0; t < 4; ++t)
      o[t] = canon(gf * __uint_as_float((__float_as_uint(y[4 + t]) & him) | (__float_as_uint(y[t]) & ~him)));
    __builtin_amdgcn_raw_buffer_store_b128(o, rws, WS_H3 + c * 8192 + (g & 1) * 4096 + (128 * w + 8 * cc + 4 * (g >> 1)) * 4, 0, AUX_SC1);
  }
  STAMP(13);
  {
    const int off = WS_H3 + (tid >> 1) * 8192 + (c >> 7) * 4096 + (c & 127) * 32 + (tid & 1) * 16;
    v4u y = __builtin_amdgcn_raw_buffer_load_b128(rws, off, 0, AUX_SC1);
    unsigned spins = 0;
    for (;;) {
      if (!__any(has_sent(y)) || ++spins > SPIN_CAP) break;
      __builtin_amdgcn_s_sleep(1);
      asm volatile("" ::: "memory");
      y = __builtin_amdgcn_raw_buffer_load_b128(rws, opq(off), 0, AUX_SC1);
    }
    float v[4];
#pragma unroll
    for (int i = 0; i < 4; ++i) {
      v[i] = __uint_as_float(y[i]);
      v[i] += dpp<XOR2>(v[i]); v[i] += dpp<ROR4>(v[i]); v[i] += dpp<ROR8>(v[i]);
      v[i] = xrow_sum(v[i]);
    }
    if (lane < 2) *(LAS f32x4*)(L + L_FN + (w * 8 + 4 * lane) * 4) = f32x4{v[0], v[1], v[2], v[3]};
  }
  STAMP(14);
  LDS_BARRIER();
  if (tid < 8) {
    float s = 0.f;
#pragma unroll
    for (int ww = 0; ww < 8; ++ww) s += *(LAS float*)(L + L_FN + (ww * 8 + tid) * 4);
    out[(c >> 7) * DM + (c & 127) * 8 + tid] = (unsigned short)(pk_bf16(s, 0.f) & 0xFFFFu);
  }
}

}

extern "C" void kernel_launch(void* const* d_in, const int* in_sizes, int n_in, void* d_out, int out_size, void* d_ws, size_t ws_size,
                              hipStream_t stream) {
  (void)in_sizes; (void)n_in; (void)out_size; (void)ws_size;
  typedef const unsigned short* P;
  gated_mix_attn_decode_kernel<<<NWG, NTHR, 0, stream>>>((P)d_in[0], (P)d_in[1], (P)d_in[2], (P)d_in[3], (P)d_in[4], (P)d_in[6], (P)d_in[7],
                                                         (P)d_in[8], (P)d_in[9], (P)d_in[10], (unsigned short*)d_out, (unsigned char*)d_ws);
}
```

```cpp
#include <hip/hip_runtime.h>
#include <hip/hip_bf16.h>
#include <stdint.h>

#define GAS __attribute__((address_space(1)))
#define LAS __attribute__((address_space(3)))
typedef unsigned v4u __attribute__((ext_vector_type(4)));
typedef unsigned v2u __attribute__((ext_vector_type(2)));
typedef float f32x4 __attribute__((ext_vector_type(4)));
typedef short bf16x8 __attribute__((ext_vector_type(8)));
typedef __bf16 bf16x2_t __attribute__((ext_vector_type(2)));
typedef unsigned long long u64;

namespace {

constexpr int DM = 1024, HKV = 4, SK = 32768, DH = 128, WIN = 4096, NQ = 4096;
constexpr int NWG = 256, NTHR = 512;
constexpr float C2 = 0.08838834764831845f * 1.4426950408889634f;

constexpr int WS_EPOCH = 0;
constexpr int WS_FLAGS = 1024;
constexpr int WS_QG = 4096;
constexpr int WS_H2 = WS_QG + 2 * 2 * 2 * 1024 * 16;
constexpr int WS_H3 = WS_H2 + NWG * 8192;
constexpr int WS_END = WS_H3 + NWG * 8192;

constexpr int AUX_NT = 2, AUX_SC1 = 16;
constexpr unsigned SENT = 0xFFFFFFFFu;
constexpr unsigned SPIN_CAP = 1u << 17;

constexpr int PSTR = 132;
constexpr int L_PART = 0;
constexpr int L_ML = L_PART + 2 * 8 * 8 * PSTR * 4;
constexpr int L_Q = L_ML + 2 * 8 * 8 * 8;
constexpr int L_QP = L_Q + 2 * 8 * 136 * 2;
constexpr int L_GP = L_QP + 8 * 128 * 4;
constexpr int L_CB = L_GP + 64;
constexpr int L_CM = L_CB + 8 * 32 * 4;
constexpr int L_FN = L_CM + 64;
constexpr int L_END = L_FN + 8 * 32 * 4;

__device__ __forceinline__ f32x4 mfma(v4u a, v4u b, f32x4 c) {
  return __builtin_amdgcn_mfma_f32_16x16x32_bf16(__builtin_bit_cast(bf16x8, a), __builtin_bit_cast(bf16x8, b), c, 0, 0, 0);
}
__device__ __forceinline__ float dot2(unsigned a, unsigned b, float c) {
  return __builtin_amdgcn_fdot2_f32_bf16(__builtin_bit_cast(bf16x2_t, a), __builtin_bit_cast(bf16x2_t, b), c, false);
}
__device__ __forceinline__ unsigned pk_bf16(float lo, float hi) {
  bf16x2_t r; r.x = (__bf16)lo; r.y = (__bf16)hi;
  return __builtin_bit_cast(unsigned, r);
}
__device__ __forceinline__ unsigned perm_lo(unsigned hi_tok, unsigned lo_tok) { return __builtin_amdgcn_perm(hi_tok, lo_tok, 0x05040100u); }
__device__ __forceinline__ unsigned perm_hi(unsigned hi_tok, unsigned lo_tok) { return __builtin_amdgcn_perm(hi_tok, lo_tok, 0x07060302u); }
template <int CTRL> __device__ __forceinline__ float dpp(float x) {
  return __builtin_bit_cast(float, __builtin_amdgcn_mov_dpp(__builtin_bit_cast(int, x), CTRL, 0xf, 0xf, true));
}
constexpr int ROR4 = 0x124, ROR8 = 0x128, XOR1 = 0xB1, XOR2 = 0x4E, HMIR = 0x141;
__device__ __forceinline__ float swap32_add(float a, float b) {
  auto r = __builtin_amdgcn_permlane32_swap(__float_as_uint(a), __float_as_uint(b), false, false);
  return __uint_as_float(r[0]) + __uint_as_float(r[1]);
}
__device__ __forceinline__ float swap16_add(float a, float b) {
  auto r = __builtin_amdgcn_permlane16_swap(__float_as_uint(a), __float_as_uint(b), false, false);
  return __uint_as_float(r[0]) + __uint_as_float(r[1]);
}
__device__ __forceinline__ float xrow_max(float x) {
  auto s = __builtin_amdgcn_permlane16_swap(__float_as_uint(x), __float_as_uint(x), false, false);
  x = fmaxf(__uint_as_float(s[0]), __uint_as_float(s[1]));
  auto t = __builtin_amdgcn_permlane32_swap(__float_as_uint(x), __float_as_uint(x), false, false);
  return fmaxf(__uint_as_float(t[0]), __uint_as_float(t[1]));
}
__device__ __forceinline__ float xrow_sum(float x) { x = swap16_add(x, x); return swap32_add(x, x); }
__device__ __forceinline__ float ex2(float x) { return __builtin_amdgcn_exp2f(x); }

#define LDS_BARRIER() asm volatile("s_waitcnt lgkmcnt(0)\n\ts_barrier" ::: "memory")
#define RAW_BARRIER() asm volatile("s_barrier" ::: "memory")
#define PIN() __builtin_amdgcn_sched_barrier(0)

__device__ __forceinline__ int opq(int v) { asm volatile("" : "+v"(v)); return v; }

typedef __amdgpu_buffer_rsrc_t rsrc_t;
__device__ __forceinline__ rsrc_t mk(const void* p, int bytes) { return __builtin_amdgcn_make_buffer_rsrc((void*)p, 0, bytes, 0x00020000); }
__device__ __forceinline__ v4u ldnt(rsrc_t r, int voff, int soff) { return __builtin_amdgcn_raw_buffer_load_b128(r, voff, soff, AUX_NT); }

template <int NT> __device__ __forceinline__ void issue_k(v4u (&K)[4 * NT], rsrc_t r, int voff, int soff) {
  voff = opq(voff);
#pragma unroll
  for (int i = 0; i < 4 * NT; ++i) K[i] = ldnt(r, voff + (i & 3) * 64, soff + (i >> 2) * 4096);
  PIN();
}
template <int NT> __device__ __forceinline__ void issue_v(v4u (&V)[4 * NT], rsrc_t r, int voff, int soff) {
  voff = opq(voff);
#pragma unroll
  for (int i = 0; i < 4 * NT; ++i) V[i] = ldnt(r, voff + (i & 3) * 256, soff + (i >> 2) * 4096);
  PIN();
}

template <int NT, bool FIRST>
__device__ __forceinline__ v4u qk_softmax(const v4u (&K)[4 * NT], const v4u (&qf)[4], float& m, float& l, f32x4 (&O)[8]) {
  f32x4 s[NT];
#pragma unroll
  for (int t = 0; t < NT; ++t) {
    s[t] = f32x4{0.f, 0.f, 0.f, 0.f};
#pragma unroll
    for (int kk = 0; kk < 4; ++kk) s[t] = mfma(K[4 * t + kk], qf[kk], s[t]);
  }
  float mx = fmaxf(fmaxf(s[0][0], s[0][1]), fmaxf(s[0][2], s[0][3]));
  if (NT == 2) mx = fmaxf(mx, fmaxf(fmaxf(s[NT - 1][0], s[NT - 1][1]), fmaxf(s[NT - 1][2], s[NT - 1][3])));
  mx = xrow_max(mx);
  const float mn = FIRST ? mx : fmaxf(m, mx);
  const float mc = (mn == -INFINITY) ? 0.f : mn * C2;
  float p[4 * NT], sum = 0.f;
#pragma unroll
  for (int t = 0; t < NT; ++t)
#pragma unroll
    for (int i = 0; i < 4; ++i) { p[4 * t + i] = ex2(fmaf(s[t][i], C2, -mc)); sum += p[4 * t + i]; }
  if (FIRST) l = sum;
  else {
    const float alpha = ex2(fmaf(m, C2, -mc));
    l = fmaf(l, alpha, sum);
#pragma unroll
    for (int e = 0; e < 8; ++e) O[e] *= alpha;
  }
  m = mn;
  v4u P;
  P.x = pk_bf16(p[0], p[1]); P.y = pk_bf16(p[2], p[3]);
  if (NT == 2) { P.z = pk_bf16(p[4 * NT - 4], p[4 * NT - 3]); P.w = pk_bf16(p[4 * NT - 2], p[4 * NT - 1]); }
  else { P.z = 0u; P.w = 0u; }
  return P;
}
template <int NT, bool FIRST>
__device__ __forceinline__ void pv(const v4u (&V)[4 * NT], v4u P, f32x4 (&O)[8]) {
#pragma unroll
  for (int e = 0; e < 8; ++e) {
    const int wd = e >> 1;
    v4u f;
    if (e & 1) {
      f.x = perm_hi(V[1][wd], V[0][wd]); f.y = perm_hi(V[3][wd], V[2][wd]);
      if (NT == 2) { f.z = perm_hi(V[4 * NT - 3][wd], V[4 * NT - 4][wd]); f.w = perm_hi(V[4 * NT - 1][wd], V[4 * NT - 2][wd]); }
      else { f.z = 0u; f.w = 0u; }
    } else {
      f.x = perm_lo(V[1][wd], V[0][wd]); f.y = perm_lo(V[3][wd], V[2][wd]);
      if (NT == 2) { f.z = perm_lo(V[4 * NT - 3][wd], V[4 * NT - 4][wd]); f.w = perm_lo(V[4 * NT - 1][wd], V[4 * NT - 2][wd]); }
      else { f.z = 0u; f.w = 0u; }
    }
    O[e] = mfma(f, P, FIRST ? f32x4{0.f, 0.f, 0.f, 0.f} : O[e]);
  }
}
__device__ __forceinline__ void dump_partial(LAS unsigned char* L, int br, int w, int g, int cc, float m, float l, const f32x4 (&O)[8]) {
  l = xrow_sum(l);
  if (cc < 8) {
    LAS float* rec = (LAS float*)(L + L_PART) + ((br * 8 + w) * 8 + cc) * PSTR + 32 * g;
#pragma unroll
    for (int i = 0; i < 4; ++i) {
      *(LAS f32x4*)(rec + 8 * i) = f32x4{O[0][i], O[1][i], O[2][i], O[3][i]};
      *(LAS f32x4*)(rec + 8 * i + 4) = f32x4{O[4][i], O[5][i], O[6][i], O[7][i]};
    }
    if (g == 0) {
      LAS float* ml = (LAS float*)(L + L_ML) + ((br * 8 + w) * 8 + cc) * 2;
      ml[0] = m * C2; ml[1] = l;
    }
  }
}
__device__ __forceinline__ void load_qf(LAS unsigned char* L, int br, int g, int cc, v4u (&qf)[4]) {
  LAS unsigned char* q = L + L_Q + ((br * 8 + (cc & 7)) * 136 + 8 * g) * 2;
#pragma unroll
  for (int kk = 0; kk < 4; ++kk) qf[kk] = *(LAS v4u*)(q + kk * 64);
}
__device__ __forceinline__ unsigned canon(float v) { const unsigned u = __float_as_uint(v); return u < 0xFFFFFFF8u ? u : 0xFFFFFFF8u; }
__device__ __forceinline__ bool has_sent(v4u v) {
  const unsigned a = v.x > v.y ? v.x : v.y, b = v.z > v.w ? v.z : v.w;
  return (a > b ? a : b) == SENT;
}
__device__ __forceinline__ u64 mix64(u64 z) {
  z = (z ^ (z >> 30)) * 0xBF58476D1CE4E5B9ull;
  z = (z ^ (z >> 27)) * 0x94D049BB133111EBull;
  return z ^ (z >> 31);
}

#define STAMP(i) do { } while (0)

__global__ void __launch_bounds__(NTHR, 2)
gated_mix_attn_decode_kernel(const unsigned short* __restrict__ x, const unsigned short* __restrict__ kA, const unsigned short* __restrict__ vA,
                             const unsigned short* __restrict__ wqA, const unsigned short* __restrict__ woA,
                             const unsigned short* __restrict__ kB, const unsigned short* __restrict__ vB,
                             const unsigned short* __restrict__ wqB, const unsigned short* __restrict__ woB,
                             const unsigned short* __restrict__ gate_w, unsigned short* __restrict__ out, unsigned char* ws) {
  __shared__ __attribute__((aligned(16))) unsigned char smem[L_END];
  LAS unsigned char* L = (LAS unsigned char*)smem;
  const int tid = threadIdx.x, lane = tid & 63, w = __builtin_amdgcn_readfirstlane(tid >> 6), c = blockIdx.x;
  const int g = lane >> 4, cc = lane & 15;
  STAMP(0);
  const int b = c >> 7, kvh = (c >> 5) & 3, sp = c & 31;
  const int col0 = kvh * 1024 + (sp & 15) * 64, kh = sp >> 4;
  const int bro = c >> 7, orow0 = (c & 127) * 32;
  const bool late = bro != 0;

  const rsrc_t rws = mk(ws, WS_END);
  const rsrc_t rwq = mk(bro ? wqB : wqA, DM * NQ * 2);
  const rsrc_t rkA = mk(kA, 2 * HKV * SK * DH * 2), rvA = mk(vA, 2 * HKV * SK * DH * 2);
  const rsrc_t rkB = mk(kB, 2 * HKV * SK * DH * 2), rvB = mk(vB, 2 * HKV * SK * DH * 2);

  const v2u epoch_v = __builtin_amdgcn_raw_buffer_load_b64(rws, opq(WS_EPOCH), 0, 0);

  auto h2slot = [&](int br, int h, int part) {
    const int cons = br * 128 + (kvh * 8 + h) * 4 + (part >> 3);
    return WS_H2 + cons * 8192 + (b * 32 + ((sp + cons) & 31)) * 128 + (part & 7) * 16;
  };
  __builtin_amdgcn_raw_buffer_store_b128(v4u{SENT, SENT, SENT, SENT}, rws, h2slot(tid >> 8, (tid >> 5) & 7, tid & 31), 0, AUX_SC1);
  PIN();
  const int rr = lane >> 3, cq = lane & 7;
  const v4u x0 = *(const GAS v4u*)(x + 512 * kh + 64 * w + 8 * rr);
  const v4u x1 = *(const GAS v4u*)(x + DM + 512 * kh + 64 * w + 8 * rr);
  const unsigned gx0 = *(const GAS unsigned*)(x + 2 * tid), gx1 = *(const GAS unsigned*)(x + DM + 2 * tid), gw = *(const GAS unsigned*)(gate_w + 2 * tid);
  const int kvo = g * 16 + cc * 256;
  const int vvo = g * 4 * 256 + cc * 16;
  const int baseA = (((b * HKV + kvh) * SK) + (SK - WIN) + sp * 128 + w * 16) * (DH * 2);
  const int baseB = (((b * HKV + kvh) * SK) + sp * 1024 + w * 32) * (DH * 2);
  constexpr int USTR = 256 * DH * 2;
  v4u Wq[8], KA[4], VA[4], K0[8], V0[8], K1[8], V1[8];
  {
    const int voff = (512 * kh + 64 * w + 8 * rr) * (NQ * 2) + col0 * 2 + cq * 16;
#pragma unroll
    for (int j = 0; j < 8; ++j) Wq[j] = ldnt(rwq, voff, j * (NQ * 2));
  }
  PIN();
  RAW_BARRIER();
  issue_k<1>(KA, rkA, kvo, baseA);
  issue_v<1>(VA, rvA, vvo, baseA);
  STAMP(1);
  {
    float a0[8], a1[8];
#pragma unroll
    for (int e = 0; e < 8; ++e) { a0[e] = 0.f; a1[e] = 0.f; }
#pragma unroll
    for (int jp = 0; jp < 4; ++jp) {
#pragma unroll
      for (int d = 0; d < 4; ++d) {
        const unsigned lo = perm_lo(Wq[2 * jp + 1][d], Wq[2 * jp][d]), hi = perm_hi(Wq[2 * jp + 1][d], Wq[2 * jp][d]);
        a0[2 * d] = dot2(lo, x0[jp], a0[2 * d]); a0[2 * d + 1] = dot2(hi, x0[jp], a0[2 * d + 1]);
        a1[2 * d] = dot2(lo, x1[jp], a1[2 * d]); a1[2 * d + 1] = dot2(hi, x1[jp], a1[2 * d + 1]);
      }
    }
    float r1[8], r2[4];
#pragma unroll
    for (int e = 0; e < 8; ++e) r1[e] = swap32_add(a0[e], a1[e]);
#pragma unroll
    for (int e = 0; e < 4; ++e) { r2[e] = swap16_add(r1[e], r1[e + 4]); r2[e] += dpp<ROR8>(r2[e]); }
    float gs = swap32_add(dot2(gw, gx0, 0.f), dot2(gw, gx1, 0.f));
    gs = swap16_add(gs, gs);
    gs += dpp<ROR8>(gs); gs += dpp<ROR4>(gs); gs += dpp<XOR2>(gs); gs += dpp<XOR1>(gs);
    if ((cc >> 3) == 0)
      *(LAS f32x4*)(L + L_QP + (w * 128 + (g >> 1) * 64 + 8 * cq + 4 * (g & 1)) * 4) = f32x4{r2[0], r2[1], r2[2], r2[3]};
    if ((lane & 31) == 0) *(LAS float*)(L + L_GP + (w * 2 + (lane >> 5)) * 4) = gs;
  }
  STAMP(2);
  LDS_BARRIER();
  const u64 epoch = ((u64)(unsigned)__builtin_amdgcn_readfirstlane(epoch_v.y) << 32) | (unsigned)__builtin_amdgcn_readfirstlane(epoch_v.x);
  const u64 tag = mix64(epoch + 0x9E3779B97F4A7C15ull);
  const unsigned thi = (unsigned)(tag >> 32), tlo = (unsigned)tag;
  const unsigned tg0 = tlo & 0xFFFu, tg1 = (tlo >> 12) & 0xFFFu, tg2 = thi & 0xFFFu, tg3 = (thi >> 12) & 0xFFFu;
  if (w == 0 && lane < 32) {
    f32x4 q = {0.f, 0.f, 0.f, 0.f};
#pragma unroll
    for (int ww = 0; ww < 8; ++ww) q += *(LAS f32x4*)(L + L_QP + (ww * 128 + lane * 4) * 4);
    const v4u gr = {((__float_as_uint(q[0]) + 0x800u) & ~0xFFFu) | tg0, ((__float_as_uint(q[1]) + 0x800u) & ~0xFFFu) | tg1,
                    ((__float_as_uint(q[2]) + 0x800u) & ~0xFFFu) | tg2, ((__float_as_uint(q[3]) + 0x800u) & ~0xFFFu) | tg3};
    __builtin_amdgcn_raw_buffer_store_b128(gr, rws, WS_QG + (((bro * 2 + (lane >> 4)) * 2 + kh) * 1024 + (col0 >> 2) + (lane & 15)) * 16, 0, AUX_SC1);
  }
  PIN();
  __builtin_amdgcn_raw_buffer_store_b128(v4u{SENT, SENT, SENT, SENT}, rws, WS_H3 + c * 8192 + tid * 16, 0, AUX_SC1);
  PIN();
  STAMP(3);
  issue_k<2>(K0, rkB, kvo, baseB);
  issue_v<2>(V0, rvB, vvo, baseB);
  __builtin_amdgcn_s_setprio(3);
  RAW_BARRIER();
  const int qoff = WS_QG + ((((tid >> 8) * 2 + b) * 2) * 1024 + kvh * 256 + (tid & 255)) * 16;
  v4u ga = __builtin_amdgcn_raw_buffer_load_b128(rws, qoff, 0, AUX_SC1);
  v4u gb = __builtin_amdgcn_raw_buffer_load_b128(rws, qoff, 1024 * 16, AUX_SC1);
  PIN();
  __builtin_amdgcn_s_setprio(0);
  issue_k<2>(K1, rkB, kvo, baseB + USTR);
  issue_v<2>(V1, rvB, vvo, baseB + USTR);
  STAMP(4);
  {
    auto stale = [&](const v4u& v) { return (((v.x ^ tg0) | (v.y ^ tg1) | (v.z ^ tg2) | (v.w ^ tg3)) & 0xFFFu) != 0u; };
    if (__any(stale(ga) || stale(gb))) {
      unsigned spins = 0;
      for (;;) {
        __builtin_amdgcn_s_sleep(2);
        asm volatile("" ::: "memory");
        const int qo = opq(qoff);
        if (stale(ga)) ga = __builtin_amdgcn_raw_buffer_load_b128(rws, qo, 0, AUX_SC1);
        if (stale(gb)) gb = __builtin_amdgcn_raw_buffer_load_b128(rws, qo, 1024 * 16, AUX_SC1);
        if (!__any(stale(ga) || stale(gb)) || ++spins > SPIN_CAP) break;
      }
    }
    float q[4];
#pragma unroll
    for (int i = 0; i < 4; ++i) q[i] = __uint_as_float(ga[i] & ~0xFFFu) + __uint_as_float(gb[i] & ~0xFFFu);
    *(LAS v2u*)(L + L_Q + (((tid >> 8) * 8 + ((tid >> 5) & 7)) * 136 + 4 * (tid & 31)) * 2) = v2u{pk_bf16(q[0], q[1]), pk_bf16(q[2], q[3])};
  }
  STAMP(5);
  LDS_BARRIER();
  if (tid == 0) __builtin_amdgcn_raw_buffer_store_b64(v2u{tlo, thi}, rws, WS_FLAGS + c * 8, 0, AUX_SC1);
  STAMP(6);

  float gf;
  {
    float z = 0.f;
#pragma unroll
    for (int ww = 0; ww < 8; ++ww) z += *(LAS float*)(L + L_GP + (ww * 2 + (g & 1)) * 4);
    const float gate = 1.f / (1.f + __expf(-z));
    gf = bro ? 1.f - gate : gate;
    asm volatile("" : "+v"(gf));
  }
  auto merge_publish = [&](int br) {
    const int ln = opq(lane), half = ln >> 5, part = ln & 31;
    LAS float* ml = (LAS float*)(L + L_ML) + ((br * 8 + 4 * half) * 8 + w) * 2;
    LAS float* rec = (LAS float*)(L + L_PART) + ((br * 8 + 4 * half) * 8 + w) * PSTR + 4 * part;
    float mw[4], lw[4];
    f32x4 aw[4];
#pragma unroll
    for (int k = 0; k < 4; ++k) { const v2u t = *(LAS v2u*)(ml + k * 16); mw[k] = __uint_as_float(t.x); lw[k] = __uint_as_float(t.y); aw[k] = *(LAS f32x4*)(rec + k * 8 * PSTR); }
    float M = fmaxf(fmaxf(mw[0], mw[1]), fmaxf(mw[2], mw[3]));
    { auto t = __builtin_amdgcn_permlane32_swap(__float_as_uint(M), __float_as_uint(M), false, false); M = fmaxf(__uint_as_float(t[0]), __uint_as_float(t[1])); }
    const float Ms = (M == -INFINITY) ? 0.f : M;
    float Ls = 0.f;
    f32x4 acc = {0.f, 0.f, 0.f, 0.f};
#pragma unroll
    for (int k = 0; k < 4; ++k) { const float f = ex2(mw[k] - Ms); Ls = fmaf(f, lw[k], Ls); acc += f * aw[k]; }
    Ls = swap32_add(Ls, Ls);
    const unsigned by = ((part & 4) ? __float_as_uint(Ls) : __float_as_uint(M)) >> (8 * (part & 3));
    v4u o;
#pragma unroll
    for (int i = 0; i < 4; ++i) o[i] = (canon(swap32_add(acc[i], acc[i])) & ~3u) | ((by >> (2 * i)) & 3u);
    if (half == 0)
      __builtin_amdgcn_raw_buffer_store_b128(o, rws, h2slot(br, w, part), 0, AUX_SC1);
  };
  auto consume_project = [&](v4u pay, const v4u (&Wf)[8]) {
    const int poff = WS_H2 + c * 8192 + tid * 16;
    unsigned spins = 0;
    for (;;) {
      const bool bad = has_sent(pay);
      if (!__any(bad) || ++spins > SPIN_CAP) break;
      __builtin_amdgcn_s_sleep(1);
      asm volatile("" ::: "memory");
      if (bad) pay = __builtin_amdgcn_raw_buffer_load_b128(rws, opq(poff), 0, AUX_SC1);
    }
    const int ln = opq(lane);
    unsigned wd = ((pay.x & 3u) | ((pay.y & 3u) << 2) | ((pay.z & 3u) << 4) | ((pay.w & 3u) << 6)) << (8 * (ln & 3));
    wd |= __builtin_amdgcn_mov_dpp(wd, XOR1, 0xf, 0xf, true);
    wd |= __builtin_amdgcn_mov_dpp(wd, XOR2, 0xf, 0xf, true);
    const unsigned ot = __builtin_amdgcn_mov_dpp(wd, HMIR, 0xf, 0xf, true);
    const float Ms = __uint_as_float((ln & 4) ? ot : wd), Lp = __uint_as_float((ln & 4) ? wd : ot);
    float M = fmaxf(Ms, dpp<ROR8>(Ms));
    M = xrow_max(M);
    const float Mz = (M == -INFINITY) ? 0.f : M;
    const float f = ex2(Ms - Mz);
    float Lw = f * Lp;
    Lw += dpp<ROR8>(Lw); Lw = xrow_sum(Lw);
    float v[4];
#pragma unroll
    for (int i = 0; i < 4; ++i) {
      v[i] = f * __uint_as_float(pay[i]);
      v[i] += dpp<ROR8>(v[i]);
      v[i] = xrow_sum(v[i]);
    }
    if (ln < 8) *(LAS f32x4*)(L + L_CB + (w * 32 + 4 * ln) * 4) = f32x4{v[0], v[1], v[2], v[3]};
    if (ln == 8) *(LAS v2u*)(L + L_CM + w * 8) = v2u{__float_as_uint(M), __float_as_uint(Lw)};
    STAMP(12);
    LDS_BARRIER();
    const int ob = (ln >> 2) & 1, g2 = ln >> 4;
    LAS float* cb = (LAS float*)(L + L_CB) + ob * 128 + 8 * g2;
    float mk_[4], lk_[4];
    f32x4 a0[4], a1[4];
#pragma unroll
    for (int k = 0; k < 4; ++k) {
      const v2u t = *(LAS v2u*)(L + L_CM + (4 * ob + k) * 8); mk_[k] = __uint_as_float(t.x); lk_[k] = __uint_as_float(t.y);
      a0[k] = *(LAS f32x4*)(cb + k * 32); a1[k] = *(LAS f32x4*)(cb + k * 32 + 4);
    }
    const float M2 = fmaxf(fmaxf(mk_[0], mk_[1]), fmaxf(mk_[2], mk_[3])), M2z = (M2 == -INFINITY) ? 0.f : M2;
    float Lt = 0.f;
    f32x4 o0 = {0.f, 0.f, 0.f, 0.f}, o1 = {0.f, 0.f, 0.f, 0.f};
#pragma unroll
    for (int k = 0; k < 4; ++k) { const float fk = ex2(mk_[k] - M2z); Lt = fmaf(fk, lk_[k], Lt); o0 += fk * a0[k]; o1 += fk * a1[k]; }
    const float inv = 1.f / Lt;
    o0 *= inv; o1 *= inv;
    const v4u of = {pk_bf16(o0[0], o0[1]), pk_bf16(o0[2], o0[3]), pk_bf16(o1[0], o1[1]), pk_bf16(o1[2], o1[3])};
    float y[8];
#pragma unroll
    for (int e = 0; e < 8; ++e) y[e] = mfma(of, Wf[e], f32x4{0.f, 0.f, 0.f, 0.f})[0];
    const unsigned him = g2 >= 2 ? ~0u : 0u;
    v4u o;
#pragma unroll
    for (int t = 0; t < 4; ++t) o[t] = canon(gf * __uint_as_float((__float_as_uint(y[4 + t]) & him) | (__float_as_uint(y[t]) & ~him)));
    __builtin_amdgcn_raw_buffer_store_b128(o, rws, WS_H3 + c * 8192 + (((g2 & 1) * 32 + 4 * w + ((ln & 15) >> 2) + c) & 63) * 128 + ((ln & 3) * 8 + 4 * (g2 >> 1)) * 4, 0, AUX_SC1);
  };
  auto issue_slot = [&](v4u (&T)[8], bool isv) {
    const rsrc_t r = mk(isv ? (const void*)vB : (bro ? (const void*)woB : (const void*)woA), isv ? 2 * HKV * SK * DH * 2 : NQ * DM * 2);
    const int voff = opq(isv ? vvo : (orow0 + 8 * g) * (DM * 2) + w * 256 + cc * 16), s0 = isv ? baseB + 3 * USTR : 0;
#pragma unroll
    for (int j = 0; j < 8; ++j) T[j] = ldnt(r, voff, s0 + (isv ? (j >> 2) * 4096 + (j & 3) * 256 : j * (DM * 2)));
    PIN();
  };
  auto wo_frags = [&](const v4u (&Wo)[8], v4u (&Wf)[8]) {
#pragma unroll
    for (int e = 0; e < 8; ++e) {
      const int wd = e >> 1;
      if (e & 1) Wf[e] = v4u{perm_hi(Wo[1][wd], Wo[0][wd]), perm_hi(Wo[3][wd], Wo[2][wd]), perm_hi(Wo[5][wd], Wo[4][wd]), perm_hi(Wo[7][wd], Wo[6][wd])};
      else Wf[e] = v4u{perm_lo(Wo[1][wd], Wo[0][wd]), perm_lo(Wo[3][wd], Wo[2][wd]), perm_lo(Wo[5][wd], Wo[4][wd]), perm_lo(Wo[7][wd], Wo[6][wd])};
    }
  };

  float m, l;
  f32x4 O[8];
  v4u qf[4];
  load_qf(L, 0, g, cc, qf);
  {
    const v4u P = qk_softmax<1, true>(KA, qf, m, l, O);
    pv<1, true>(VA, P, O);
    dump_partial(L, 0, w, g, cc, m, l, O);
  }
  PIN();
  v4u K2[8], V2[8], K3[8], SA[8], SB[8], Wf[8];
  issue_k<2>(K2, rkB, kvo, baseB + 2 * USTR);
  STAMP(7);
  load_qf(L, 1, g, cc, qf);
  {
    const v4u P = qk_softmax<2, true>(K0, qf, m, l, O);
    PIN();
    issue_v<2>(V2, rvB, vvo, baseB + 2 * USTR);
    pv<2, true>(V0, P, O);
  }
  LDS_BARRIER();
  merge_publish(0);
  PIN();
  issue_k<2>(K3, rkB, kvo, baseB + 3 * USTR);
  v4u pay = {0u, 0u, 0u, 0u};
  {
    const v4u P = qk_softmax<2, false>(K1, qf, m, l, O);
    pv<2, false>(V1, P, O);
    PIN();
    issue_slot(SA, late);
    pay = __builtin_amdgcn_raw_buffer_load_b128(rws, WS_H2 + c * 8192 + tid * 16, 0, AUX_SC1);
    issue_slot(SB, !late);
  }
  STAMP(8);
  {
    const v4u P = qk_softmax<2, false>(K2, qf, m, l, O);
    pv<2, false>(V2, P, O);
  }
  STAMP(9);
  v2u fl = {0u, 0u};
  if (c < 64) fl = __builtin_amdgcn_raw_buffer_load_b64(rws, WS_FLAGS + (tid & 255) * 8, 0, AUX_SC1);
  {
    const v4u P = qk_softmax<2, false>(K3, qf, m, l, O);
    if (late) pv<2, false>(SA, P, O); else pv<2, false>(SB, P, O);
    dump_partial(L, 1, w, g, cc, m, l, O);
  }
  STAMP(10);
  LDS_BARRIER();
  merge_publish(1);
  STAMP(11);
  if (late) { pay = __builtin_amdgcn_raw_buffer_load_b128(rws, opq(WS_H2 + c * 8192 + tid * 16), 0, AUX_SC1); wo_frags(SB, Wf); }
  else wo_frags(SA, Wf);
  consume_project(pay, Wf);
  STAMP(13);
  if (c < 64) {
    const int off = WS_H3 + (tid >> 3) * 8192 + ((c + (tid >> 3)) & 63) * 128 + (tid & 7) * 16;
    unsigned spins = 0;
    for (;;) {
      const bool bad = fl.x != tlo || fl.y != thi;
      if (!__any(bad) || ++spins > SPIN_CAP) break;
      __builtin_amdgcn_s_sleep(2);
      asm volatile("" ::: "memory");
      if (bad) fl = __builtin_amdgcn_raw_buffer_load_b64(rws, opq(WS_FLAGS + (tid & 255) * 8), 0, AUX_SC1);
    }
    LDS_BARRIER();
    if (c == 0 && tid == 0) __hip_atomic_store((GAS u64*)(ws + WS_EPOCH), epoch + 0x9E3779B97F4A7C15ull, __ATOMIC_RELAXED, __HIP_MEMORY_SCOPE_AGENT);
    v4u y[4];
#pragma unroll
    for (int k = 0; k < 4; ++k) y[k] = __builtin_amdgcn_raw_buffer_load_b128(rws, off, k * 64 * 8192, AUX_SC1);
    spins = 0;
    for (;;) {
      const bool b0 = has_sent(y[0]), b1 = has_sent(y[1]), b2 = has_sent(y[2]), b3 = has_sent(y[3]);
      if (!__any(b0 || b1 || b2 || b3) || ++spins > SPIN_CAP) break;
      __builtin_amdgcn_s_sleep(1);
      asm volatile("" ::: "memory");
      const int o2 = opq(off);
      if (b0) y[0] = __builtin_amdgcn_raw_buffer_load_b128(rws, o2, 0 * 64 * 8192, AUX_SC1);
      if (b1) y[1] = __builtin_amdgcn_raw_buffer_load_b128(rws, o2, 1 * 64 * 8192, AUX_SC1);
      if (b2) y[2] = __builtin_amdgcn_raw_buffer_load_b128(rws, o2, 2 * 64 * 8192, AUX_SC1);
      if (b3) y[3] = __builtin_amdgcn_raw_buffer_load_b128(rws, o2, 3 * 64 * 8192, AUX_SC1);
    }
    float v[4];
#pragma unroll
    for (int i = 0; i < 4; ++i) {
      v[i] = (__uint_as_float(y[0][i]) + __uint_as_float(y[1][i])) + (__uint_as_float(y[2][i]) + __uint_as_float(y[3][i]));
      v[i] += dpp<ROR8>(v[i]);
      v[i] = xrow_sum(v[i]);
    }
    if (lane < 8) *(LAS f32x4*)(L + L_FN + (w * 32 + 4 * lane) * 4) = f32x4{v[0], v[1], v[2], v[3]};
    STAMP(14);
    LDS_BARRIER();
    if (tid < 32) {
      float s = 0.f;
#pragma unroll
      for (int ww = 0; ww < 8; ++ww) s += *(LAS float*)(L + L_FN + (ww * 32 + tid) * 4);
      out[(c >> 5) * DM + (c & 31) * 32 + tid] = (unsigned short)(pk_bf16(s, 0.f) & 0xFFFFu);
    }
  }
}

}

extern "C" void kernel_launch(void* const* d_in, const int* in_sizes, int n_in, void* d_out, int out_size, void* d_ws, size_t ws_size,
                              hipStream_t stream) {
  (void)in_sizes; (void)n_in; (void)out_size; (void)ws_size;
  typedef const unsigned short* P;
  gated_mix_attn_decode_kernel<<<NWG, NTHR, 0, stream>>>((P)d_in[0], (P)d_in[1], (P)d_in[2], (P)d_in[3], (P)d_in[4], (P)d_in[6], (P)d_in[7],
                                                         (P)d_in[8], (P)d_in[9], (P)d_in[10], (unsigned short*)d_out, (unsigned char*)d_ws);
}
```

```cpp
#include <hip/hip_runtime.h>
#include <hip/hip_bf16.h>
#include <stdint.h>

#define GAS __attribute__((address_space(1)))
#define LAS __attribute__((address_space(3)))
typedef unsigned v4u __attribute__((ext_vector_type(4)));
typedef unsigned v2u __attribute__((ext_vector_type(2)));
typedef float f32x4 __attribute__((ext_vector_type(4)));
typedef short bf16x8 __attribute__((ext_vector_type(8)));
typedef __bf16 bf16x2_t __attribute__((ext_vector_type(2)));
typedef unsigned long long u64;

namespace {

constexpr int DM = 1024, HKV = 4, SK = 32768, DH = 128, WIN = 4096, NQ = 4096;
constexpr int NWG = 256, NTHR = 512;
constexpr float C2 = 0.08838834764831845f * 1.4426950408889634f;

constexpr int WS_EPOCH = 0;
constexpr int WS_QG = 4096;
constexpr int WS_H2 = WS_QG + 2 * 2 * 2 * 1024 * 16;
constexpr int WS_H3 = WS_H2 + NWG * 8192;
constexpr int WS_END = WS_H3 + NWG * 8192;

constexpr int AUX_NT = 2, AUX_SC1 = 16;
constexpr unsigned SPIN_CAP = 1u << 17;

constexpr int PSTR = 132;
constexpr int L_PART = 0;
constexpr int L_ML = L_PART + 2 * 8 * 8 * PSTR * 4;
constexpr int L_Q = L_ML + 2 * 8 * 8 * 8;
constexpr int L_QP = L_Q + 2 * 8 * 136 * 2;
constexpr int L_GP = L_QP + 8 * 128 * 4;
constexpr int L_CB = L_GP + 64;
constexpr int L_CM = L_CB + 8 * 32 * 4;
constexpr int L_FN = L_CM + 64;
constexpr int L_END = L_FN + 8 * 32 * 4;

__device__ __forceinline__ f32x4 mfma(v4u a, v4u b, f32x4 c) {
  return __builtin_amdgcn_mfma_f32_16x16x32_bf16(__builtin_bit_cast(bf16x8, a), __builtin_bit_cast(bf16x8, b), c, 0, 0, 0);
}
__device__ __forceinline__ float dot2(unsigned a, unsigned b, float c) {
  return __builtin_amdgcn_fdot2_f32_bf16(__builtin_bit_cast(bf16x2_t, a), __builtin_bit_cast(bf16x2_t, b), c, false);
}
__device__ __forceinline__ unsigned pk_bf16(float lo, float hi) {
  bf16x2_t r; r.x = (__bf16)lo; r.y = (__bf16)hi;
  return __builtin_bit_cast(unsigned, r);
}
__device__ __forceinline__ unsigned perm_lo(unsigned hi_tok, unsigned lo_tok) { return __builtin_amdgcn_perm(hi_tok, lo_tok, 0x05040100u); }
__device__ __forceinline__ unsigned perm_hi(unsigned hi_tok, unsigned lo_tok) { return __builtin_amdgcn_perm(hi_tok, lo_tok, 0x07060302u); }
template <int CTRL> __device__ __forceinline__ float dpp(float x) {
  return __builtin_bit_cast(float, __builtin_amdgcn_mov_dpp(__builtin_bit_cast(int, x), CTRL, 0xf, 0xf, true));
}
constexpr int ROR4 = 0x124, ROR8 = 0x128, XOR1 = 0xB1, XOR2 = 0x4E, HMIR = 0x141;
__device__ __forceinline__ float swap32_add(float a, float b) {
  auto r = __builtin_amdgcn_permlane32_swap(__float_as_uint(a), __float_as_uint(b), false, false);
  return __uint_as_float(r[0]) + __uint_as_float(r[1]);
}
__device__ __forceinline__ float swap16_add(float a, float b) {
  auto r = __builtin_amdgcn_permlane16_swap(__float_as_uint(a), __float_as_uint(b), false, false);
  return __uint_as_float(r[0]) + __uint_as_float(r[1]);
}
__device__ __forceinline__ float xrow_max(float x) {
  auto s = __builtin_amdgcn_permlane16_swap(__float_as_uint(x), __float_as_uint(x), false, false);
  x = fmaxf(__uint_as_float(s[0]), __uint_as_float(s[1]));
  auto t = __builtin_amdgcn_permlane32_swap(__float_as_uint(x), __float_as_uint(x), false, false);
  return fmaxf(__uint_as_float(t[0]), __uint_as_float(t[1]));
}
__device__ __forceinline__ float xrow_sum(float x) { x = swap16_add(x, x); return swap32_add(x, x); }
__device__ __forceinline__ float ex2(float x) { return __builtin_amdgcn_exp2f(x); }

#define LDS_BARRIER() asm volatile("s_waitcnt lgkmcnt(0)\n\ts_barrier" ::: "memory")
#define RAW_BARRIER() asm volatile("s_barrier" ::: "memory")
#define PIN() __builtin_amdgcn_sched_barrier(0)

__device__ __forceinline__ int opq(int v) { asm volatile("" : "+v"(v)); return v; }

typedef __amdgpu_buffer_rsrc_t rsrc_t;
__device__ __forceinline__ rsrc_t mk(const void* p, int bytes) { return __builtin_amdgcn_make_buffer_rsrc((void*)p, 0, bytes, 0x00020000); }
__device__ __forceinline__ v4u ldnt(rsrc_t r, int voff, int soff) { return __builtin_amdgcn_raw_buffer_load_b128(r, voff, soff, AUX_NT); }

template <int NT> __device__ __forceinline__ void issue_k(v4u (&K)[4 * NT], rsrc_t r, int voff, int soff) {
  voff = opq(voff);
#pragma unroll
  for (int i = 0; i < 4 * NT; ++i) K[i] = ldnt(r, voff + (i & 3) * 64, soff + (i >> 2) * 4096);
  PIN();
}
template <int NT> __device__ __forceinline__ void issue_v(v4u (&V)[4 * NT], rsrc_t r, int voff, int soff) {
  voff = opq(voff);
#pragma unroll
  for (int i = 0; i < 4 * NT; ++i) V[i] = ldnt(r, voff + (i & 3) * 256, soff + (i >> 2) * 4096);
  PIN();
}

template <int NT, bool FIRST>
__device__ __forceinline__ v4u qk_softmax(const v4u (&K)[4 * NT], const v4u (&qf)[4], float& m, float& l, f32x4 (&O)[8]) {
  f32x4 s[NT];
#pragma unroll
  for (int t = 0; t < NT; ++t) {
    s[t] = f32x4{0.f, 0.f, 0.f, 0.f};
#pragma unroll
    for (int kk = 0; kk < 4; ++kk) s[t] = mfma(K[4 * t + kk], qf[kk], s[t]);
  }
  float mx = fmaxf(fmaxf(s[0][0], s[0][1]), fmaxf(s[0][2], s[0][3]));
  if (NT == 2) mx = fmaxf(mx, fmaxf(fmaxf(s[NT - 1][0], s[NT - 1][1]), fmaxf(s[NT - 1][2], s[NT - 1][3])));
  mx = xrow_max(mx);
  const float mn = FIRST ? mx : fmaxf(m, mx);
  const float mc = (mn == -INFINITY) ? 0.f : mn * C2;
  float p[4 * NT], sum = 0.f;
#pragma unroll
  for (int t = 0; t < NT; ++t)
#pragma unroll
    for (int i = 0; i < 4; ++i) { p[4 * t + i] = ex2(fmaf(s[t][i], C2, -mc)); sum += p[4 * t + i]; }
  if (FIRST) l = sum;
  else {
    const float alpha = ex2(fmaf(m, C2, -mc));
    l = fmaf(l, alpha, sum);
#pragma unroll
    for (int e = 0; e < 8; ++e) O[e] *= alpha;
  }
  m = mn;
  asm volatile("" : "+v"(l));
  v4u P;
  P.x = pk_bf16(p[0], p[1]); P.y = pk_bf16(p[2], p[3]);
  if (NT == 2) { P.z = pk_bf16(p[4 * NT - 4], p[4 * NT - 3]); P.w = pk_bf16(p[4 * NT - 2], p[4 * NT - 1]); }
  else { P.z = 0u; P.w = 0u; }
  return P;
}
template <int NT, bool FIRST>
__device__ __forceinline__ void pv(const v4u (&V)[4 * NT], v4u P, f32x4 (&O)[8]) {
#pragma unroll
  for (int e = 0; e < 8; ++e) {
    const int wd = e >> 1;
    v4u f;
    if (e & 1) {
      f.x = perm_hi(V[1][wd], V[0][wd]); f.y = perm_hi(V[3][wd], V[2][wd]);
      if (NT == 2) { f.z = perm_hi(V[4 * NT - 3][wd], V[4 * NT - 4][wd]); f.w = perm_hi(V[4 * NT - 1][wd], V[4 * NT - 2][wd]); }
      else { f.z = 0u; f.w = 0u; }
    } else {
      f.x = perm_lo(V[1][wd], V[0][wd]); f.y = perm_lo(V[3][wd], V[2][wd]);
      if (NT == 2) { f.z = perm_lo(V[4 * NT - 3][wd], V[4 * NT - 4][wd]); f.w = perm_lo(V[4 * NT - 1][wd], V[4 * NT - 2][wd]); }
      else { f.z = 0u; f.w = 0u; }
    }
    O[e] = mfma(f, P, FIRST ? f32x4{0.f, 0.f, 0.f, 0.f} : O[e]);
  }
}
__device__ __forceinline__ void dump_partial(LAS unsigned char* L, int br, int w, int g, int cc, float m, float l, const f32x4 (&O)[8]) {
  l = xrow_sum(l);
  if (cc < 8) {
    LAS float* rec = (LAS float*)(L + L_PART) + ((br * 8 + w) * 8 + cc) * PSTR + 32 * g;
#pragma unroll
    for (int e = 0; e < 8; ++e) *(LAS f32x4*)(rec + 4 * e) = O[e];
    if (g == 0) *(LAS v2u*)(L + L_ML + ((br * 8 + w) * 8 + cc) * 8) = v2u{__float_as_uint(m * C2), __float_as_uint(l)};
  }
}
__device__ __forceinline__ void load_qf(LAS unsigned char* L, int br, int g, int cc, v4u (&qf)[4]) {
  LAS unsigned char* q = L + L_Q + ((br * 8 + (cc & 7)) * 136 + 8 * g) * 2;
#pragma unroll
  for (int kk = 0; kk < 4; ++kk) qf[kk] = *(LAS v4u*)(q + kk * 64);
}
__device__ __forceinline__ v4u put_low(float a, float b, float c, float d, unsigned lb) {
  return v4u{__builtin_amdgcn_perm(__float_as_uint(a) + 0x80u, lb, 0x07060500u), __builtin_amdgcn_perm(__float_as_uint(b) + 0x80u, lb, 0x07060501u),
             __builtin_amdgcn_perm(__float_as_uint(c) + 0x80u, lb, 0x07060502u), __builtin_amdgcn_perm(__float_as_uint(d) + 0x80u, lb, 0x07060503u)};
}
__device__ __forceinline__ unsigned get_low(const v4u& v) {
  return __builtin_amdgcn_perm(__builtin_amdgcn_perm(v.w, v.z, 0x0c0c0400u), __builtin_amdgcn_perm(v.y, v.x, 0x0c0c0400u), 0x05040100u);
}
__device__ __forceinline__ float hi24(unsigned u) { return __uint_as_float(u & ~0xFFu); }
__device__ __forceinline__ bool line_bad(unsigned diff) {
  diff |= __builtin_amdgcn_mov_dpp(diff, XOR1, 0xf, 0xf, true);
  diff |= __builtin_amdgcn_mov_dpp(diff, XOR2, 0xf, 0xf, true);
  diff |= __builtin_amdgcn_mov_dpp(diff, HMIR, 0xf, 0xf, true);
  return diff != 0u;
}
__device__ __forceinline__ u64 mix64(u64 z) {
  z = (z ^ (z >> 30)) * 0xBF58476D1CE4E5B9ull;
  z = (z ^ (z >> 27)) * 0x94D049BB133111EBull;
  return z ^ (z >> 31);
}

#define STAMP(i) do { } while (0)

__global__ void __launch_bounds__(NTHR, 2)
gated_mix_attn_decode_kernel(const unsigned short* __restrict__ x, const unsigned short* __restrict__ kA, const unsigned short* __restrict__ vA,
                             const unsigned short* __restrict__ wqA, const unsigned short* __restrict__ woA,
                             const unsigned short* __restrict__ kB, const unsigned short* __restrict__ vB,
                             const unsigned short* __restrict__ wqB, const unsigned short* __restrict__ woB,
                             const unsigned short* __restrict__ gate_w, unsigned short* __restrict__ out, unsigned char* ws) {
  __shared__ __attribute__((aligned(16))) unsigned char smem[L_END];
  LAS unsigned char* L = (LAS unsigned char*)smem;
  const int tid = threadIdx.x, lane = tid & 63, w = __builtin_amdgcn_readfirstlane(tid >> 6), c = blockIdx.x;
  const int g = lane >> 4, cc = lane & 15;
  STAMP(0);
  const int b = c >> 7, kvh = (c >> 5) & 3, sp = c & 31;
  const int col0 = kvh * 1024 + (sp & 15) * 64, kh = sp >> 4;
  const int bro = c >> 7, orow0 = (c & 127) * 32;
  const bool late = bro != 0;

  const rsrc_t rws = mk(ws, WS_END);
  const rsrc_t rwq = mk(bro ? wqB : wqA, DM * NQ * 2);
  const rsrc_t rkA = mk(kA, 2 * HKV * SK * DH * 2), rvA = mk(vA, 2 * HKV * SK * DH * 2);
  const rsrc_t rkB = mk(kB, 2 * HKV * SK * DH * 2), rvB = mk(vB, 2 * HKV * SK * DH * 2);

  const v2u epoch_v = __builtin_amdgcn_raw_buffer_load_b64(rws, opq(WS_EPOCH), 0, 0);

  auto h2slot = [&](int br, int h, int part) {
    const int cons = br * 128 + (kvh * 8 + h) * 4 + (part >> 3);
    return WS_H2 + cons * 8192 + (b * 32 + ((sp + cons) & 31)) * 128 + (part & 7) * 16;
  };
  const int rr = lane >> 3, cq = lane & 7;
  const v4u x0 = *(const GAS v4u*)(x + 512 * kh + 64 * w + 8 * rr);
  const v4u x1 = *(const GAS v4u*)(x + DM + 512 * kh + 64 * w + 8 * rr);
  const unsigned gx0 = *(const GAS unsigned*)(x + 2 * tid), gx1 = *(const GAS unsigned*)(x + DM + 2 * tid), gw = *(const GAS unsigned*)(gate_w + 2 * tid);
  const int kvo = g * 16 + cc * 256;
  const int vvo = g * 4 * 256 + cc * 16;
  const int baseA = (((b * HKV + kvh) * SK) + (SK - WIN) + sp * 128 + w * 16) * (DH * 2);
  const int baseB = (((b * HKV + kvh) * SK) + sp * 1024 + w * 32) * (DH * 2);
  constexpr int USTR = 256 * DH * 2;
  v4u Wq[8], KA[4], VA[4], K0[8], V0[8], K1[8], V1[8];
  {
    const int voff = (512 * kh + 64 * w + 8 * rr) * (NQ * 2) + col0 * 2 + cq * 16;
#pragma unroll
    for (int j = 0; j < 8; ++j) Wq[j] = ldnt(rwq, voff, j * (NQ * 2));
  }
  PIN();
  RAW_BARRIER();
  issue_k<1>(KA, rkA, kvo, baseA);
  issue_v<1>(VA, rvA, vvo, baseA);
  STAMP(1);
  {
    float a0[8], a1[8];
#pragma unroll
    for (int e = 0; e < 8; ++e) { a0[e] = 0.f; a1[e] = 0.f; }
#pragma unroll
    for (int jp = 0; jp < 4; ++jp) {
#pragma unroll
      for (int d = 0; d < 4; ++d) {
        const unsigned lo = perm_lo(Wq[2 * jp + 1][d], Wq[2 * jp][d]), hi = perm_hi(Wq[2 * jp + 1][d], Wq[2 * jp][d]);
        a0[2 * d] = dot2(lo, x0[jp], a0[2 * d]); a0[2 * d + 1] = dot2(hi, x0[jp], a0[2 * d + 1]);
        a1[2 * d] = dot2(lo, x1[jp], a1[2 * d]); a1[2 * d + 1] = dot2(hi, x1[jp], a1[2 * d + 1]);
      }
    }
    float r1[8], r2[4];
#pragma unroll
    for (int e = 0; e < 8; ++e) r1[e] = swap32_add(a0[e], a1[e]);
#pragma unroll
    for (int e = 0; e < 4; ++e) { r2[e] = swap16_add(r1[e], r1[e + 4]); r2[e] += dpp<ROR8>(r2[e]); }
    float gs = swap32_add(dot2(gw, gx0, 0.f), dot2(gw, gx1, 0.f));
    gs = swap16_add(gs, gs);
    gs += dpp<ROR8>(gs); gs += dpp<ROR4>(gs); gs += dpp<XOR2>(gs); gs += dpp<XOR1>(gs);
    if ((cc >> 3) == 0)
      *(LAS f32x4*)(L + L_QP + (w * 128 + (g >> 1) * 64 + 8 * cq + 4 * (g & 1)) * 4) = f32x4{r2[0], r2[1], r2[2], r2[3]};
    if ((lane & 31) == 0) *(LAS float*)(L + L_GP + (w * 2 + (lane >> 5)) * 4) = gs;
  }
  STAMP(2);
  LDS_BARRIER();
  const u64 epoch = ((u64)(unsigned)__builtin_amdgcn_readfirstlane(epoch_v.y) << 32) | (unsigned)__builtin_amdgcn_readfirstlane(epoch_v.x);
  const u64 tag = mix64(epoch + 0x9E3779B97F4A7C15ull);
  const unsigned thi = (unsigned)(tag >> 32), tlo = (unsigned)tag;
  const unsigned tw = (lane & 1) ? thi : tlo;
  const unsigned tg0 = tlo & 0xFFFu, tg1 = (tlo >> 12) & 0xFFFu, tg2 = thi & 0xFFFu, tg3 = (thi >> 12) & 0xFFFu;
  if (w == 0 && lane < 32) {
    f32x4 q = {0.f, 0.f, 0.f, 0.f};
#pragma unroll
    for (int ww = 0; ww < 8; ++ww) q += *(LAS f32x4*)(L + L_QP + (ww * 128 + lane * 4) * 4);
    const v4u gr = {((__float_as_uint(q[0]) + 0x800u) & ~0xFFFu) | tg0, ((__float_as_uint(q[1]) + 0x800u) & ~0xFFFu) | tg1,
                    ((__float_as_uint(q[2]) + 0x800u) & ~0xFFFu) | tg2, ((__float_as_uint(q[3]) + 0x800u) & ~0xFFFu) | tg3};
    __builtin_amdgcn_raw_buffer_store_b128(gr, rws, WS_QG + (((bro * 2 + (lane >> 4)) * 2 + kh) * 1024 + (col0 >> 2) + (lane & 15)) * 16, 0, AUX_SC1);
  }
  PIN();
  STAMP(3);
  issue_k<2>(K0, rkB, kvo, baseB);
  issue_v<2>(V0, rvB, vvo, baseB);
  __builtin_amdgcn_s_setprio(3);
  RAW_BARRIER();
  const int qoff = WS_QG + ((((tid >> 8) * 2 + b) * 2) * 1024 + kvh * 256 + (tid & 255)) * 16;
  v4u ga = __builtin_amdgcn_raw_buffer_load_b128(rws, qoff, 0, AUX_SC1);
  v4u gb = __builtin_amdgcn_raw_buffer_load_b128(rws, qoff, 1024 * 16, AUX_SC1);
  PIN();
  __builtin_amdgcn_s_setprio(0);
  issue_k<2>(K1, rkB, kvo, baseB + USTR);
  issue_v<2>(V1, rvB, vvo, baseB + USTR);
  STAMP(4);
  {
    auto stale = [&](const v4u& v) { return (((v.x ^ tg0) | (v.y ^ tg1) | (v.z ^ tg2) | (v.w ^ tg3)) & 0xFFFu) != 0u; };
    if (__any(stale(ga) || stale(gb))) {
      unsigned spins = 0;
      for (;;) {
        __builtin_amdgcn_s_sleep(2);
        asm volatile("" ::: "memory");
        const int qo = opq(qoff);
        if (stale(ga)) ga = __builtin_amdgcn_raw_buffer_load_b128(rws, qo, 0, AUX_SC1);
        if (stale(gb)) gb = __builtin_amdgcn_raw_buffer_load_b128(rws, qo, 1024 * 16, AUX_SC1);
        if (!__any(stale(ga) || stale(gb)) || ++spins > SPIN_CAP) break;
      }
    }
    float q[4];
#pragma unroll
    for (int i = 0; i < 4; ++i) q[i] = __uint_as_float(ga[i] & ~0xFFFu) + __uint_as_float(gb[i] & ~0xFFFu);
    *(LAS v2u*)(L + L_Q + (((tid >> 8) * 8 + ((tid >> 5) & 7)) * 136 + 4 * (tid & 31)) * 2) = v2u{pk_bf16(q[0], q[1]), pk_bf16(q[2], q[3])};
  }
  STAMP(5);
  LDS_BARRIER();
  STAMP(6);

  float gf;
  {
    float z = 0.f;
#pragma unroll
    for (int ww = 0; ww < 8; ++ww) z += *(LAS float*)(L + L_GP + (ww * 2 + (g & 1)) * 4);
    const float gate = 1.f / (1.f + __expf(-z));
    gf = bro ? 1.f - gate : gate;
    asm volatile("" : "+v"(gf));
  }
  auto merge_publish = [&](int a_ml, int a_rec, int a_out, unsigned a_sel) {
    LAS unsigned char* ml = L + a_ml;
    LAS unsigned char* rec = L + a_rec;
    float mw[4], lw[4];
    f32x4 aw[4];
#pragma unroll
    for (int k = 0; k < 4; ++k) { const v2u t = *(LAS v2u*)(ml + k * 64); mw[k] = __uint_as_float(t.x); lw[k] = __uint_as_float(t.y); aw[k] = *(LAS f32x4*)(rec + k * 8 * PSTR * 4); }
    float M = fmaxf(fmaxf(mw[0], mw[1]), fmaxf(mw[2], mw[3]));
    { auto t = __builtin_amdgcn_permlane32_swap(__float_as_uint(M), __float_as_uint(M), false, false); M = fmaxf(__uint_as_float(t[0]), __uint_as_float(t[1])); }
    const float Ms = (M == -INFINITY) ? 0.f : M;
    float Ls = 0.f;
    f32x4 acc = {0.f, 0.f, 0.f, 0.f};
#pragma unroll
    for (int k = 0; k < 4; ++k) { const float f = ex2(mw[k] - Ms); Ls = fmaf(f, lw[k], Ls); acc += f * aw[k]; }
    Ls = swap32_add(Ls, Ls);
    const unsigned by = __builtin_amdgcn_perm(__float_as_uint(Ls), __float_as_uint(M), a_sel);
    const unsigned lb = (tw & 0xFCFCFCFCu) | ((by | (by << 6) | (by << 12) | (by << 18)) & 0x03030303u);
    const v4u o = put_low(swap32_add(acc[0], acc[0]), swap32_add(acc[1], acc[1]), swap32_add(acc[2], acc[2]), swap32_add(acc[3], acc[3]), lb);
    __builtin_amdgcn_raw_buffer_store_b128(o, rws, a_out, 0, AUX_SC1);
  };
  auto merge_addr = [&](int br, int& a_ml, int& a_rec, int& a_out, unsigned& a_sel) {
    const int ln = opq(lane), half = ln >> 5, part = ln & 31;
    a_ml = opq(L_ML + ((br * 8 + 4 * half) * 8 + w) * 8);
    a_rec = opq(L_PART + (((br * 8 + 4 * half) * 8 + w) * PSTR + 4 * part) * 4);
    a_out = opq(half ? 0x7FFFFFF0 : h2slot(br, w, part));
    a_sel = (unsigned)opq(0x0C0C0C00 | (part & 7));
  };
  auto consume_project = [&](v4u pay, const v4u (&Wf)[8], int poff, int h3off) {
    unsigned spins = 0, lowb;
    for (;;) {
      lowb = get_low(pay);
      const bool bad = line_bad((lowb ^ tw) & 0xFCFCFCFCu);
      if (!__any(bad) || ++spins > SPIN_CAP) break;
      __builtin_amdgcn_s_sleep(1);
      asm volatile("" ::: "memory");
      if (bad) pay = __builtin_amdgcn_raw_buffer_load_b128(rws, opq(poff), 0, AUX_SC1);
    }
    const int ln = opq(lane);
    lowb &= 0x03030303u;
    unsigned wd = ((lowb | (lowb >> 6) | (lowb >> 12) | (lowb >> 18)) & 0xFFu) << (8 * (ln & 3));
    wd |= __builtin_amdgcn_mov_dpp(wd, XOR1, 0xf, 0xf, true);
    wd |= __builtin_amdgcn_mov_dpp(wd, XOR2, 0xf, 0xf, true);
    const unsigned ot = __builtin_amdgcn_mov_dpp(wd, HMIR, 0xf, 0xf, true);
    const float Ms = __uint_as_float((ln & 4) ? ot : wd), Lp = __uint_as_float((ln & 4) ? wd : ot);
    float M = fmaxf(Ms, dpp<ROR8>(Ms));
    M = xrow_max(M);
    const float Mz = (M == -INFINITY) ? 0.f : M;
    const float f = ex2(Ms - Mz);
    float Lw = f * Lp;
    Lw += dpp<ROR8>(Lw); Lw = xrow_sum(Lw);
    float v[4];
#pragma unroll
    for (int i = 0; i < 4; ++i) {
      v[i] = f * hi24(pay[i]);
      v[i] += dpp<ROR8>(v[i]);
      v[i] = xrow_sum(v[i]);
    }
    if (ln < 8) *(LAS f32x4*)(L + L_CB + (w * 32 + 4 * ln) * 4) = f32x4{v[0], v[1], v[2], v[3]};
    if (ln == 8) *(LAS v2u*)(L + L_CM + w * 8) = v2u{__float_as_uint(M), __float_as_uint(Lw)};
    STAMP(12);
    LDS_BARRIER();
    const int ob = (ln >> 2) & 1, g2 = ln >> 4;
    LAS float* cb = (LAS float*)(L + L_CB) + ob * 128 + 8 * g2;
    float mk_[4], lk_[4];
    f32x4 a0[4], a1[4];
#pragma unroll
    for (int k = 0; k < 4; ++k) {
      const v2u t = *(LAS v2u*)(L + L_CM + (4 * ob + k) * 8); mk_[k] = __uint_as_float(t.x); lk_[k] = __uint_as_float(t.y);
      a0[k] = *(LAS f32x4*)(cb + k * 32); a1[k] = *(LAS f32x4*)(cb + k * 32 + 4);
    }
    const float M2 = fmaxf(fmaxf(mk_[0], mk_[1]), fmaxf(mk_[2], mk_[3])), M2z = (M2 == -INFINITY) ? 0.f : M2;
    float Lt = 0.f;
    f32x4 o0 = {0.f, 0.f, 0.f, 0.f}, o1 = {0.f, 0.f, 0.f, 0.f};
#pragma unroll
    for (int k = 0; k < 4; ++k) { const float fk = ex2(mk_[k] - M2z); Lt = fmaf(fk, lk_[k], Lt); o0 += fk * a0[k]; o1 += fk * a1[k]; }
    const float inv = __builtin_amdgcn_rcpf(Lt);
    o0 *= inv; o1 *= inv;
    const v4u of = {pk_bf16(o0[0], o0[1]), pk_bf16(o0[2], o0[3]), pk_bf16(o1[0], o1[1]), pk_bf16(o1[2], o1[3])};
    float y[8];
#pragma unroll
    for (int e = 0; e < 8; ++e) y[e] = mfma(of, Wf[e], f32x4{0.f, 0.f, 0.f, 0.f})[0];
    const unsigned him = g2 >= 2 ? ~0u : 0u;
    float ys[4];
#pragma unroll
    for (int t = 0; t < 4; ++t) ys[t] = gf * __uint_as_float((__float_as_uint(y[4 + t]) & him) | (__float_as_uint(y[t]) & ~him));
    const v4u o = put_low(ys[0], ys[1], ys[2], ys[3], (g2 >> 1) ? thi : tlo);
    __builtin_amdgcn_raw_buffer_store_b128(o, rws, h3off, 0, AUX_SC1);
  };
  auto issue_wo = [&](v4u (&T)[8]) {
    const rsrc_t r = mk(bro ? woB : woA, NQ * DM * 2);
    const int voff = opq((orow0 + 2 * g) * (DM * 2) + w * 256 + cc * 16);
#pragma unroll
    for (int j = 0; j < 8; ++j) T[j] = ldnt(r, voff, (8 * (j & 3) + (j >> 2)) * (DM * 2));
    PIN();
  };
  auto wo_frags = [&](const v4u (&Wo)[8], v4u (&Wf)[8]) {
#pragma unroll
    for (int e = 0; e < 8; ++e) {
      const int wd = e >> 1;
      if (e & 1) Wf[e] = v4u{perm_hi(Wo[1][wd], Wo[0][wd]), perm_hi(Wo[3][wd], Wo[2][wd]), perm_hi(Wo[5][wd], Wo[4][wd]), perm_hi(Wo[7][wd], Wo[6][wd])};
      else Wf[e] = v4u{perm_lo(Wo[1][wd], Wo[0][wd]), perm_lo(Wo[3][wd], Wo[2][wd]), perm_lo(Wo[5][wd], Wo[4][wd]), perm_lo(Wo[7][wd], Wo[6][wd])};
    }
  };

  float m, l;
  f32x4 O[8];
  v4u qf[4];
  load_qf(L, 0, g, cc, qf);
  {
    const v4u P = qk_softmax<1, true>(KA, qf, m, l, O);
    pv<1, true>(VA, P, O);
    dump_partial(L, 0, w, g, cc, m, l, O);
  }
  PIN();
  v4u K2[8], V2[8], K3[8], V3[8], Wo[8], Wf[8];
  issue_k<2>(K2, rkB, kvo, baseB + 2 * USTR);
  STAMP(7);
  load_qf(L, 1, g, cc, qf);
  {
    const v4u P = qk_softmax<2, true>(K0, qf, m, l, O);
    PIN();
    issue_v<2>(V2, rvB, vvo, baseB + 2 * USTR);
    pv<2, true>(V0, P, O);
  }
  LDS_BARRIER();
  { int a0, a1, a2; unsigned a3; merge_addr(0, a0, a1, a2, a3); merge_publish(a0, a1, a2, a3); }
  PIN();
  issue_k<2>(K3, rkB, kvo, baseB + 3 * USTR);
  v4u pay = {0u, 0u, 0u, 0u};
  {
    const v4u P = qk_softmax<2, false>(K1, qf, m, l, O);
    pv<2, false>(V1, P, O);
    PIN();
    issue_v<2>(V3, rvB, vvo, baseB + 3 * USTR);
    pay = __builtin_amdgcn_raw_buffer_load_b128(rws, WS_H2 + c * 8192 + tid * 16, 0, AUX_SC1);
    RAW_BARRIER();
    issue_wo(Wo);
  }
  int a_ml, a_rec, a_out; unsigned a_sel;
  merge_addr(1, a_ml, a_rec, a_out, a_sel);
  const int poff = opq(WS_H2 + c * 8192 + tid * 16);
  const int h3off = opq(WS_H3 + c * 8192 + (((g & 1) * 32 + 4 * w + (cc >> 2) + c) & 63) * 128 + ((cc & 3) * 8 + 4 * (g >> 1)) * 4);
  const int yoff = opq(WS_H3 + (tid >> 3) * 8192 + ((c + (tid >> 3)) & 63) * 128 + (tid & 7) * 16);
  STAMP(8);
  {
    const v4u P = qk_softmax<2, false>(K2, qf, m, l, O);
    pv<2, false>(V2, P, O);
  }
  STAMP(9);
  {
    const v4u P = qk_softmax<2, false>(K3, qf, m, l, O);
    pv<2, false>(V3, P, O);
    dump_partial(L, 1, w, g, cc, m, l, O);
  }
  STAMP(10);
  LDS_BARRIER();
  merge_publish(a_ml, a_rec, a_out, a_sel);
  STAMP(11);
  if (late) pay = __builtin_amdgcn_raw_buffer_load_b128(rws, poff, 0, AUX_SC1);
  wo_frags(Wo, Wf);
#pragma unroll
  for (int e = 0; e < 8; ++e) asm volatile("" : "+v"(Wf[e]));
  consume_project(pay, Wf, poff, h3off);
  STAMP(13);
  if (c < 64) {
    const int off = yoff;
    v4u y[4];
#pragma unroll
    for (int k = 0; k < 4; ++k) y[k] = __builtin_amdgcn_raw_buffer_load_b128(rws, off, k * 64 * 8192, AUX_SC1);
    unsigned spins = 0;
    for (;;) {
      const bool b0 = line_bad(get_low(y[0]) ^ tw), b1 = line_bad(get_low(y[1]) ^ tw), b2 = line_bad(get_low(y[2]) ^ tw), b3 = line_bad(get_low(y[3]) ^ tw);
      if (!__any(b0 || b1 || b2 || b3) || ++spins > SPIN_CAP) break;
      __builtin_amdgcn_s_sleep(1);
      asm volatile("" ::: "memory");
      const int o2 = opq(off);
      if (b0) y[0] = __builtin_amdgcn_raw_buffer_load_b128(rws, o2, 0 * 64 * 8192, AUX_SC1);
      if (b1) y[1] = __builtin_amdgcn_raw_buffer_load_b128(rws, o2, 1 * 64 * 8192, AUX_SC1);
      if (b2) y[2] = __builtin_amdgcn_raw_buffer_load_b128(rws, o2, 2 * 64 * 8192, AUX_SC1);
      if (b3) y[3] = __builtin_amdgcn_raw_buffer_load_b128(rws, o2, 3 * 64 * 8192, AUX_SC1);
    }
    float v[4];
#pragma unroll
    for (int i = 0; i < 4; ++i) {
      v[i] = (hi24(y[0][i]) + hi24(y[1][i])) + (hi24(y[2][i]) + hi24(y[3][i]));
      v[i] += dpp<ROR8>(v[i]);
      v[i] = xrow_sum(v[i]);
    }
    if (lane < 8) *(LAS f32x4*)(L + L_FN + (w * 32 + 4 * lane) * 4) = f32x4{v[0], v[1], v[2], v[3]};
    STAMP(14);
    LDS_BARRIER();
    if (c == 0 && tid == 32) __hip_atomic_store((GAS u64*)(ws + WS_EPOCH), epoch + 0x9E3779B97F4A7C15ull, __ATOMIC_RELAXED, __HIP_MEMORY_SCOPE_AGENT);
    if (tid < 32) {
      float s = 0.f;
#pragma unroll
      for (int ww = 0; ww < 8; ++ww) s += *(LAS float*)(L + L_FN + (ww * 32 + tid) * 4);
      out[(c >> 5) * DM + (c & 31) * 32 + tid] = (unsigned short)(pk_bf16(s, 0.f) & 0xFFFFu);
    }
  }
}

}

extern "C" void kernel_launch(void* const* d_in, const int* in_sizes, int n_in, void* d_out, int out_size, void* d_ws, size_t ws_size,
                              hipStream_t stream) {
  (void)in_sizes; (void)n_in; (void)out_size; (void)ws_size;
  typedef const unsigned short* P;
  gated_mix_attn_decode_kernel<<<NWG, NTHR, 0, stream>>>((P)d_in[0], (P)d_in[1], (P)d_in[2], (P)d_in[3], (P)d_in[4], (P)d_in[6], (P)d_in[7],
                                                         (P)d_in[8], (P)d_in[9], (P)d_in[10], (unsigned short*)d_out, (unsigned char*)d_ws);
}
```

```cpp
#include <hip/hip_runtime.h>
#include <hip/hip_bf16.h>
#include <stdint.h>

#define GAS __attribute__((address_space(1)))
#define LAS __attribute__((address_space(3)))
typedef unsigned v4u __attribute__((ext_vector_type(4)));
typedef unsigned v2u __attribute__((ext_vector_type(2)));
typedef float f32x4 __attribute__((ext_vector_type(4)));
typedef short bf16x8 __attribute__((ext_vector_type(8)));
typedef __bf16 bf16x2_t __attribute__((ext_vector_type(2)));
typedef unsigned long long u64;

namespace {

constexpr int DM = 1024, HKV = 4, SK = 32768, DH = 128, WIN = 4096, NQ = 4096;
constexpr int NWG = 256, NTHR = 512;
constexpr float C2 = 0.08838834764831845f * 1.4426950408889634f;

constexpr int WS_EPOCH = 0;
constexpr int WS_QG = 4096;
constexpr int WS_H2 = WS_QG + 2 * 2 * 2 * 1024 * 16;
constexpr int WS_H3 = WS_H2 + NWG * 8192;
constexpr int WS_END = WS_H3 + NWG * 8192;
constexpr int WS_TOTAL = WS_END;

#define POLL_SLEEP 1
constexpr int AUX_NT = 2, AUX_SC1 = 16;
constexpr int OOB = 0x7FFFF000;
constexpr unsigned SPIN_CAP = 1u << 17;

constexpr int PSTR = 132;
constexpr int L_PART = 0;
constexpr int L_ML = L_PART + 2 * 8 * 8 * PSTR * 4;
constexpr int L_Q = L_ML + 2 * 8 * 8 * 8;
constexpr int L_QP = L_Q + 2 * 8 * 136 * 2;
constexpr int L_GP = L_QP + 8 * 128 * 4;
constexpr int L_CB = L_GP + 64;
constexpr int L_CM = L_CB + 8 * 32 * 4;
constexpr int L_FN = L_CM + 64;
constexpr int L_END = L_FN + 8 * 32 * 4;

__device__ __forceinline__ f32x4 mfma(v4u a, v4u b, f32x4 c) {
  return __builtin_amdgcn_mfma_f32_16x16x32_bf16(__builtin_bit_cast(bf16x8, a), __builtin_bit_cast(bf16x8, b), c, 0, 0, 0);
}
__device__ __forceinline__ float dot2(unsigned a, unsigned b, float c) {
  return __builtin_amdgcn_fdot2_f32_bf16(__builtin_bit_cast(bf16x2_t, a), __builtin_bit_cast(bf16x2_t, b), c, false);
}
__device__ __forceinline__ unsigned pk_bf16(float lo, float hi) {
  bf16x2_t r; r.x = (__bf16)lo; r.y = (__bf16)hi;
  return __builtin_bit_cast(unsigned, r);
}
__device__ __forceinline__ unsigned perm_lo(unsigned hi_tok, unsigned lo_tok) { return __builtin_amdgcn_perm(hi_tok, lo_tok, 0x05040100u); }
__device__ __forceinline__ unsigned perm_hi(unsigned hi_tok, unsigned lo_tok) { return __builtin_amdgcn_perm(hi_tok, lo_tok, 0x07060302u); }
template <int CTRL> __device__ __forceinline__ float dpp(float x) {
  return __builtin_bit_cast(float, __builtin_amdgcn_mov_dpp(__builtin_bit_cast(int, x), CTRL, 0xf, 0xf, true));
}
constexpr int ROR4 = 0x124, ROR8 = 0x128, XOR1 = 0xB1, XOR2 = 0x4E, HMIR = 0x141;
__device__ __forceinline__ float swap32_add(float a, float b) {
  auto r = __builtin_amdgcn_permlane32_swap(__float_as_uint(a), __float_as_uint(b), false, false);
  return __uint_as_float(r[0]) + __uint_as_float(r[1]);
}
__device__ __forceinline__ float swap16_add(float a, float b) {
  auto r = __builtin_amdgcn_permlane16_swap(__float_as_uint(a), __float_as_uint(b), false, false);
  return __uint_as_float(r[0]) + __uint_as_float(r[1]);
}
__device__ __forceinline__ float xrow_max(float x) {
  auto s = __builtin_amdgcn_permlane16_swap(__float_as_uint(x), __float_as_uint(x), false, false);
  x = fmaxf(__uint_as_float(s[0]), __uint_as_float(s[1]));
  auto t = __builtin_amdgcn_permlane32_swap(__float_as_uint(x), __float_as_uint(x), false, false);
  return fmaxf(__uint_as_float(t[0]), __uint_as_float(t[1]));
}
__device__ __forceinline__ float xrow_sum(float x) { x = swap16_add(x, x); return swap32_add(x, x); }
__device__ __forceinline__ float ex2(float x) { return __builtin_amdgcn_exp2f(x); }

#define LDS_BARRIER() asm volatile("s_waitcnt lgkmcnt(0)\n\ts_barrier" ::: "memory")
#define RAW_BARRIER() asm volatile("s_barrier" ::: "memory")
#define PIN() __builtin_amdgcn_sched_barrier(0)

__device__ __forceinline__ int opq(int v) { asm volatile("" : "+v"(v)); return v; }

typedef __amdgpu_buffer_rsrc_t rsrc_t;
__device__ __forceinline__ rsrc_t mk(const void* p, int bytes) { return __builtin_amdgcn_make_buffer_rsrc((void*)p, 0, bytes, 0x00020000); }
__device__ __forceinline__ v4u ldnt(rsrc_t r, int voff, int soff) { return __builtin_amdgcn_raw_buffer_load_b128(r, voff, soff, AUX_NT); }

template <int NG> __device__ __forceinline__ void issue_k(v4u (&K)[2 * NG], rsrc_t r, int voff, int soff) {
  voff = opq(voff);
#pragma unroll
  for (int i = 0; i < 2 * NG; ++i) K[i] = ldnt(r, voff + ((i >> 1) & 1) * 2048 + (i & 1) * 128, soff + (i >> 2) * 4096);
  PIN();
}
template <int NG> __device__ __forceinline__ void issue_v(v4u (&V)[2 * NG], rsrc_t r, int voff, int soff) {
  voff = opq(voff);
#pragma unroll
  for (int i = 0; i < 2 * NG; ++i) V[i] = ldnt(r, voff + ((i % NG) & 1) * 2048 + (i / NG) * 256, soff + ((i % NG) >> 1) * 4096);
  PIN();
}
__device__ __forceinline__ float vmax(float a, float b) { float r; asm("v_max_f32 %0, %1, %2" : "=v"(r) : "v"(a), "v"(b)); return r; }
__device__ __forceinline__ float xrow_vmax(float x) {
  auto s = __builtin_amdgcn_permlane16_swap(__float_as_uint(x), __float_as_uint(x), false, false);
  x = vmax(__uint_as_float(s[0]), __uint_as_float(s[1]));
  auto t = __builtin_amdgcn_permlane32_swap(__float_as_uint(x), __float_as_uint(x), false, false);
  return vmax(__uint_as_float(t[0]), __uint_as_float(t[1]));
}

template <int NG, bool FIRST>
__device__ __forceinline__ v4u qk_softmax(const v4u (&K)[2 * NG], const v4u (&qf)[2], bool z, float& m, float& l, f32x4 (&O)[8]) {
  float s[NG];
#pragma unroll
  for (int i = 0; i < NG; ++i) {
    f32x4 d = mfma(K[2 * i], qf[0], f32x4{0.f, 0.f, 0.f, 0.f});
    d = mfma(K[2 * i + 1], qf[1], d);
    s[i] = (z ? d[3] : d[0]) + dpp<XOR1>(z ? d[1] : d[2]);
  }
  float mx = vmax(s[0], s[1]);
  if (NG == 4) mx = vmax(mx, vmax(s[NG - 2], s[NG - 1]));
  mx = vmax(mx, dpp<XOR1>(mx));
  mx = xrow_vmax(mx);
  const float mn = FIRST ? mx : vmax(m, mx);
  const float mc = (mn == -INFINITY) ? 0.f : mn * C2;
  float p[NG], sum = 0.f;
#pragma unroll
  for (int i = 0; i < NG; ++i) { p[i] = ex2(fmaf(s[i], C2, -mc)); sum += p[i]; }
  if (FIRST) l = sum;
  else {
    const float alpha = ex2(fmaf(m, C2, -mc));
    l = fmaf(l, alpha, sum);
#pragma unroll
    for (int e = 0; e < 8; ++e) O[e] *= alpha;
  }
  m = mn;
  asm volatile("" : "+v"(l));
  const unsigned p01 = pk_bf16(p[0], p[1]), p23 = NG == 4 ? pk_bf16(p[NG - 2], p[NG - 1]) : 0u;
  return v4u{z ? 0u : p01, z ? 0u : p23, z ? p01 : 0u, z ? p23 : 0u};
}
template <int NG, bool FIRST>
__device__ __forceinline__ void pv(const v4u (&V)[2 * NG], v4u P, f32x4 (&O)[8]) {
#pragma unroll
  for (int e = 0; e < 8; ++e) {
    const int wd = e >> 1;
    v4u f;
    if (e & 1) {
      f.x = perm_hi(V[1][wd], V[0][wd]); f.z = perm_hi(V[NG + 1][wd], V[NG][wd]);
      if (NG == 4) { f.y = perm_hi(V[NG - 1][wd], V[NG - 2][wd]); f.w = perm_hi(V[2 * NG - 1][wd], V[2 * NG - 2][wd]); }
      else { f.y = 0u; f.w = 0u; }
    } else {
      f.x = perm_lo(V[1][wd], V[0][wd]); f.z = perm_lo(V[NG + 1][wd], V[NG][wd]);
      if (NG == 4) { f.y = perm_lo(V[NG - 1][wd], V[NG - 2][wd]); f.w = perm_lo(V[2 * NG - 1][wd], V[2 * NG - 2][wd]); }
      else { f.y = 0u; f.w = 0u; }
    }
    O[e] = mfma(f, P, FIRST ? f32x4{0.f, 0.f, 0.f, 0.f} : O[e]);
  }
}
__device__ __forceinline__ void dump_partial(LAS unsigned char* L, int br, int w, int g, int cc, float m, float l, const f32x4 (&O)[8]) {
  l += dpp<XOR1>(l);
  l = xrow_sum(l);
  const bool z = cc & 1;
  LAS float* rec = (LAS float*)(L + L_PART) + ((br * 8 + w) * 8 + (cc >> 1)) * PSTR + 32 * g + 16 * (cc & 1);
#pragma unroll
  for (int k = 0; k < 4; ++k) {
    f32x4 t;
#pragma unroll
    for (int i = 0; i < 4; ++i) t[i] = (z ? O[4 + k][i] : O[k][i]) + dpp<XOR1>(z ? O[k][i] : O[4 + k][i]);
    *(LAS f32x4*)(rec + 4 * k) = t;
  }
  if (g == 0 && !z) *(LAS v2u*)(L + L_ML + ((br * 8 + w) * 8 + (cc >> 1)) * 8) = v2u{__float_as_uint(m * C2), __float_as_uint(l)};
}
__device__ __forceinline__ void load_qf(LAS unsigned char* L, int br, int g, int cc, v4u (&qf)[2]) {
  LAS unsigned char* q = L + L_Q + ((br * 8 + (cc >> 1)) * 136 + 16 * g + 8 * (cc & 1)) * 2;
  qf[0] = *(LAS v4u*)q; qf[1] = *(LAS v4u*)(q + 128);
}
__device__ __forceinline__ v4u put_low(float a, float b, float c, float d, unsigned lb) {
  return v4u{__builtin_amdgcn_perm(__float_as_uint(a) + 0x80u, lb, 0x07060500u), __builtin_amdgcn_perm(__float_as_uint(b) + 0x80u, lb, 0x07060501u),
             __builtin_amdgcn_perm(__float_as_uint(c) + 0x80u, lb, 0x07060502u), __builtin_amdgcn_perm(__float_as_uint(d) + 0x80u, lb, 0x07060503u)};
}
__device__ __forceinline__ unsigned get_low(const v4u& v) {
  return __builtin_amdgcn_perm(__builtin_amdgcn_perm(v.w, v.z, 0x0c0c0400u), __builtin_amdgcn_perm(v.y, v.x, 0x0c0c0400u), 0x05040100u);
}
__device__ __forceinline__ float hi24(unsigned u) { return __uint_as_float(u & ~0xFFu); }
__device__ __forceinline__ bool line_bad(unsigned diff) {
  diff |= __builtin_amdgcn_mov_dpp(diff, XOR1, 0xf, 0xf, true);
  diff |= __builtin_amdgcn_mov_dpp(diff, XOR2, 0xf, 0xf, true);
  diff |= __builtin_amdgcn_mov_dpp(diff, HMIR, 0xf, 0xf, true);
  return diff != 0u;
}
template <int N, int STR, unsigned MASK>
__device__ __forceinline__ void wait_lines(rsrc_t r, v4u (&y)[N], int off, unsigned tw) {
  for (unsigned spins = 0; spins < SPIN_CAP; ++spins) {
    bool bad[N], any = false;
#pragma unroll
    for (int k = 0; k < N; ++k) { bad[k] = line_bad((get_low(y[k]) ^ tw) & MASK); any |= bad[k]; }
    if (!__any(any)) break;
    __builtin_amdgcn_s_sleep(POLL_SLEEP);
    asm volatile("" ::: "memory");
    const int o = opq(off);
#pragma unroll
    for (int k = 0; k < N; ++k)
      if (bad[k]) y[k] = __builtin_amdgcn_raw_buffer_load_b128(r, o, k * STR, AUX_SC1);
  }
}
__device__ __forceinline__ u64 mix64(u64 z) {
  z = (z ^ (z >> 30)) * 0xBF58476D1CE4E5B9ull;
  z = (z ^ (z >> 27)) * 0x94D049BB133111EBull;
  return z ^ (z >> 31);
}

#define STAMP(i) do { } while (0)

__global__ void __launch_bounds__(NTHR, 2)
gated_mix_attn_decode_kernel(const unsigned short* __restrict__ x, const unsigned short* __restrict__ kA, const unsigned short* __restrict__ vA,
                             const unsigned short* __restrict__ wqA, const unsigned short* __restrict__ woA,
                             const unsigned short* __restrict__ kB, const unsigned short* __restrict__ vB,
                             const unsigned short* __restrict__ wqB, const unsigned short* __restrict__ woB,
                             const unsigned short* __restrict__ gate_w, unsigned short* __restrict__ out, unsigned char* ws) {
  __shared__ __attribute__((aligned(16))) unsigned char smem[L_END];
  LAS unsigned char* L = (LAS unsigned char*)smem;
  const int tid = threadIdx.x, lane = tid & 63, w = __builtin_amdgcn_readfirstlane(tid >> 6), c = blockIdx.x;
  const int g = lane >> 4, cc = lane & 15;
  STAMP(0);
  const int b = c >> 7, kvh = (c >> 5) & 3, sp = c & 31;
  const int col0 = kvh * 1024 + (sp & 15) * 64, kh = sp >> 4;
  const int bro = c >> 7, orow0 = (c & 127) * 32;
  const bool late = bro != 0;

  const rsrc_t rws = mk(ws, WS_END);
  const rsrc_t rwq = mk(bro ? wqB : wqA, DM * NQ * 2);
  const rsrc_t rkA = mk(kA, 2 * HKV * SK * DH * 2), rvA = mk(vA, 2 * HKV * SK * DH * 2);
  const rsrc_t rkB = mk(kB, 2 * HKV * SK * DH * 2), rvB = mk(vB, 2 * HKV * SK * DH * 2);

  const v2u epoch_v = __builtin_amdgcn_raw_buffer_load_b64(rws, opq(WS_EPOCH), 0, 0);

  auto h2slot = [&](int br, int h, int part) {
    const int cons = br * 128 + (kvh * 8 + h) * 4 + (part >> 3);
    return WS_H2 + cons * 8192 + (b * 32 + ((sp + cons) & 31)) * 128 + (part & 7) * 16;
  };
  const int rr = lane >> 3, cq = lane & 7;
  const v4u x0 = *(const GAS v4u*)(x + 512 * kh + 64 * w + 8 * rr);
  const v4u x1 = *(const GAS v4u*)(x + DM + 512 * kh + 64 * w + 8 * rr);
  const unsigned gx0 = *(const GAS unsigned*)(x + 2 * tid), gx1 = *(const GAS unsigned*)(x + DM + 2 * tid), gw = *(const GAS unsigned*)(gate_w + 2 * tid);
  const int kvo = (cc >> 1) * 256 + (2 * g + (cc & 1)) * 16;
  const int vvo = g * 2 * 256 + cc * 16;
  const bool zl = cc & 1;
  const int baseA = (((b * HKV + kvh) * SK) + (SK - WIN) + sp * 128 + w * 16) * (DH * 2);
  const int baseB = (((b * HKV + kvh) * SK) + sp * 1024 + w * 32) * (DH * 2);
  constexpr int USTR = 256 * DH * 2;
  v4u Wq[8], KA[4], VA[4], K0[8], V0[8], K1[8], V1[8];
  {
    const int voff = (512 * kh + 64 * w + 8 * rr) * (NQ * 2) + col0 * 2 + cq * 16;
#pragma unroll
    for (int j = 0; j < 8; ++j) Wq[j] = ldnt(rwq, voff, j * (NQ * 2));
  }
  PIN();
  RAW_BARRIER();
  issue_k<2>(KA, rkA, kvo, baseA);
  issue_v<2>(VA, rvA, vvo, baseA);
  STAMP(1);
  {
    float a0[8], a1[8];
#pragma unroll
    for (int e = 0; e < 8; ++e) { a0[e] = 0.f; a1[e] = 0.f; }
#pragma unroll
    for (int jp = 0; jp < 4; ++jp) {
#pragma unroll
      for (int d = 0; d < 4; ++d) {
        const unsigned lo = perm_lo(Wq[2 * jp + 1][d], Wq[2 * jp][d]), hi = perm_hi(Wq[2 * jp + 1][d], Wq[2 * jp][d]);
        a0[2 * d] = dot2(lo, x0[jp], a0[2 * d]); a0[2 * d + 1] = dot2(hi, x0[jp], a0[2 * d + 1]);
        a1[2 * d] = dot2(lo, x1[jp], a1[2 * d]); a1[2 * d + 1] = dot2(hi, x1[jp], a1[2 * d + 1]);
      }
    }
    float r1[8], r2[4];
#pragma unroll
    for (int e = 0; e < 8; ++e) r1[e] = swap32_add(a0[e], a1[e]);
#pragma unroll
    for (int e = 0; e < 4; ++e) { r2[e] = swap16_add(r1[e], r1[e + 4]); r2[e] += dpp<ROR8>(r2[e]); }
    float gs = swap32_add(dot2(gw, gx0, 0.f), dot2(gw, gx1, 0.f));
    gs = swap16_add(gs, gs);
    gs += dpp<ROR8>(gs); gs += dpp<ROR4>(gs); gs += dpp<XOR2>(gs); gs += dpp<XOR1>(gs);
    if ((cc >> 3) == 0)
      *(LAS f32x4*)(L + L_QP + (w * 128 + (g >> 1) * 64 + 8 * cq + 4 * (g & 1)) * 4) = f32x4{r2[0], r2[1], r2[2], r2[3]};
    if ((lane & 31) == 0) *(LAS float*)(L + L_GP + (w * 2 + (lane >> 5)) * 4) = gs;
  }
  STAMP(2);
  LDS_BARRIER();
  const u64 epoch = ((u64)(unsigned)__builtin_amdgcn_readfirstlane(epoch_v.y) << 32) | (unsigned)__builtin_amdgcn_readfirstlane(epoch_v.x);
  const u64 tag = mix64(epoch + 0x9E3779B97F4A7C15ull);
  const unsigned thi = (unsigned)(tag >> 32), tlo = (unsigned)tag;
  const unsigned tw = (lane & 1) ? thi : tlo;
  if (w == 0 && lane < 32) {
    f32x4 q = {0.f, 0.f, 0.f, 0.f};
#pragma unroll
    for (int ww = 0; ww < 8; ++ww) q += *(LAS f32x4*)(L + L_QP + (ww * 128 + lane * 4) * 4);
    __builtin_amdgcn_raw_buffer_store_b128(put_low(q[0], q[1], q[2], q[3], tw), rws, WS_QG + (((bro * 2 + (lane >> 4)) * 2 + kh) * 1024 + (col0 >> 2) + (lane & 15)) * 16, 0, AUX_SC1);
  }
  PIN();
  STAMP(3);
  issue_k<4>(K0, rkB, kvo, baseB);
  issue_v<4>(V0, rvB, vvo, baseB);
  __builtin_amdgcn_s_setprio(3);
  RAW_BARRIER();
  const int qoff = WS_QG + ((((tid >> 8) * 2 + b) * 2) * 1024 + kvh * 256 + (tid & 255)) * 16;
  v4u ga = __builtin_amdgcn_raw_buffer_load_b128(rws, qoff, 0, AUX_SC1);
  v4u gb = __builtin_amdgcn_raw_buffer_load_b128(rws, qoff, 1024 * 16, AUX_SC1);
  PIN();
  __builtin_amdgcn_s_setprio(0);
  issue_k<4>(K1, rkB, kvo, baseB + USTR);
  issue_v<4>(V1, rvB, vvo, baseB + USTR);
  STAMP(4);
  {
    auto stale = [&](const v4u& v) { return line_bad(get_low(v) ^ tw); };
    if (__any(stale(ga) || stale(gb))) {
      unsigned spins = 0;
      for (;;) {
        __builtin_amdgcn_s_sleep(2);
        asm volatile("" ::: "memory");
        const int qo = opq(qoff);
        const bool sa = stale(ga), sb = stale(gb);
        if (sa) ga = __builtin_amdgcn_raw_buffer_load_b128(rws, qo, 0, AUX_SC1);
        if (sb) gb = __builtin_amdgcn_raw_buffer_load_b128(rws, qo, 1024 * 16, AUX_SC1);
        if (!__any(stale(ga) || stale(gb)) || ++spins > SPIN_CAP) break;
      }
    }
    float q[4];
#pragma unroll
    for (int i = 0; i < 4; ++i) q[i] = hi24(ga[i]) + hi24(gb[i]);
    *(LAS v2u*)(L + L_Q + (((tid >> 8) * 8 + ((tid >> 5) & 7)) * 136 + 4 * (tid & 31)) * 2) = v2u{pk_bf16(q[0], q[1]), pk_bf16(q[2], q[3])};
  }
  STAMP(5);
  LDS_BARRIER();
  STAMP(6);

  v4u K2[8], V2[8], K3[8], V3[8], Wo[8], Wf[8];
  issue_k<4>(K2, rkB, kvo, baseB + 2 * USTR);
  float gf;
  {
    float z = 0.f;
#pragma unroll
    for (int ww = 0; ww < 8; ++ww) z += *(LAS float*)(L + L_GP + (ww * 2 + (g & 1)) * 4);
    const float gate = 1.f / (1.f + __expf(-z));
    gf = bro ? 1.f - gate : gate;
    asm volatile("" : "+v"(gf));
  }
  auto merge_publish = [&](int a_ml, int a_rec, int a_out, unsigned a_sel) {
    LAS unsigned char* ml = L + a_ml;
    LAS unsigned char* rec = L + a_rec;
    float mw[4], lw[4];
    f32x4 aw[4];
#pragma unroll
    for (int k = 0; k < 4; ++k) { const v2u t = *(LAS v2u*)(ml + k * 64); mw[k] = __uint_as_float(t.x); lw[k] = __uint_as_float(t.y); aw[k] = *(LAS f32x4*)(rec + k * 8 * PSTR * 4); }
    float M = vmax(vmax(mw[0], mw[1]), vmax(mw[2], mw[3]));
    { auto t = __builtin_amdgcn_permlane32_swap(__float_as_uint(M), __float_as_uint(M), false, false); M = vmax(__uint_as_float(t[0]), __uint_as_float(t[1])); }
    const float Ms = (M == -INFINITY) ? 0.f : M;
    float Ls = 0.f;
    f32x4 acc = {0.f, 0.f, 0.f, 0.f};
#pragma unroll
    for (int k = 0; k < 4; ++k) { const float f = ex2(mw[k] - Ms); Ls = fmaf(f, lw[k], Ls); acc += f * aw[k]; }
    Ls = swap32_add(Ls, Ls);
    const unsigned by = __builtin_amdgcn_perm(__float_as_uint(Ls), __float_as_uint(M), a_sel);
    const unsigned lb = (tw & 0xFCFCFCFCu) | ((by | (by << 6) | (by << 12) | (by << 18)) & 0x03030303u);
    const v4u o = put_low(swap32_add(acc[0], acc[0]), swap32_add(acc[1], acc[1]), swap32_add(acc[2], acc[2]), swap32_add(acc[3], acc[3]), lb);
    __builtin_amdgcn_raw_buffer_store_b128(o, rws, a_out, 0, AUX_SC1);
  };
  auto merge_addr = [&](int br, int& a_ml, int& a_rec, int& a_out, unsigned& a_sel) {
    const int ln = opq(lane), half = ln >> 5, part = ln & 31;
    a_ml = opq(L_ML + ((br * 8 + 4 * half) * 8 + w) * 8);
    a_rec = opq(L_PART + (((br * 8 + 4 * half) * 8 + w) * PSTR + 4 * part) * 4);
    a_out = opq(half ? 0x7FFFFFF0 : h2slot(br, w, part));
    a_sel = (unsigned)opq(0x0C0C0C00 | (part & 7));
  };
  auto consume_project = [&](v4u pay, const v4u (&Wf)[8], int poff, int h3off) {
    v4u py[1] = {pay};
    wait_lines<1, 0, 0xFCFCFCFCu>(rws, py, poff, tw);
    pay = py[0];
    unsigned lowb = get_low(pay);
    const int ln = opq(lane);
    lowb &= 0x03030303u;
    unsigned wd = ((lowb | (lowb >> 6) | (lowb >> 12) | (lowb >> 18)) & 0xFFu) << (8 * (ln & 3));
    wd |= __builtin_amdgcn_mov_dpp(wd, XOR1, 0xf, 0xf, true);
    wd |= __builtin_amdgcn_mov_dpp(wd, XOR2, 0xf, 0xf, true);
    const unsigned ot = __builtin_amdgcn_mov_dpp(wd, HMIR, 0xf, 0xf, true);
    const float Ms = __uint_as_float((ln & 4) ? ot : wd), Lp = __uint_as_float((ln & 4) ? wd : ot);
    float M = vmax(Ms, dpp<ROR8>(Ms));
    M = xrow_vmax(M);
    const float Mz = (M == -INFINITY) ? 0.f : M;
    const float f = ex2(Ms - Mz);
    float Lw = f * Lp;
    Lw += dpp<ROR8>(Lw); Lw = xrow_sum(Lw);
    float v[4];
#pragma unroll
    for (int i = 0; i < 4; ++i) {
      v[i] = f * hi24(pay[i]);
      v[i] += dpp<ROR8>(v[i]);
      v[i] = xrow_sum(v[i]);
    }
    if (ln < 8) *(LAS f32x4*)(L + L_CB + (w * 32 + 4 * ln) * 4) = f32x4{v[0], v[1], v[2], v[3]};
    if (ln == 8) *(LAS v2u*)(L + L_CM + w * 8) = v2u{__float_as_uint(M), __float_as_uint(Lw)};
    STAMP(12);
    LDS_BARRIER();
    const int ob = (ln >> 2) & 1, g2 = ln >> 4;
    LAS float* cb = (LAS float*)(L + L_CB) + ob * 128 + 8 * g2;
    float mk_[4], lk_[4];
    f32x4 a0[4], a1[4];
#pragma unroll
    for (int k = 0; k < 4; ++k) {
      const v2u t = *(LAS v2u*)(L + L_CM + (4 * ob + k) * 8); mk_[k] = __uint_as_float(t.x); lk_[k] = __uint_as_float(t.y);
      a0[k] = *(LAS f32x4*)(cb + k * 32); a1[k] = *(LAS f32x4*)(cb + k * 32 + 4);
    }
    const float M2 = vmax(vmax(mk_[0], mk_[1]), vmax(mk_[2], mk_[3])), M2z = (M2 == -INFINITY) ? 0.f : M2;
    float Lt = 0.f;
    f32x4 o0 = {0.f, 0.f, 0.f, 0.f}, o1 = {0.f, 0.f, 0.f, 0.f};
#pragma unroll
    for (int k = 0; k < 4; ++k) { const float fk = ex2(mk_[k] - M2z); Lt = fmaf(fk, lk_[k], Lt); o0 += fk * a0[k]; o1 += fk * a1[k]; }
    const float inv = __builtin_amdgcn_rcpf(Lt);
    o0 *= inv; o1 *= inv;
    const v4u of = {pk_bf16(o0[0], o0[1]), pk_bf16(o0[2], o0[3]), pk_bf16(o1[0], o1[1]), pk_bf16(o1[2], o1[3])};
    float y[8];
#pragma unroll
    for (int e = 0; e < 8; ++e) y[e] = mfma(of, Wf[e], f32x4{0.f, 0.f, 0.f, 0.f})[0];
    const unsigned him = g2 >= 2 ? ~0u : 0u;
    float ys[4];
#pragma unroll
    for (int t = 0; t < 4; ++t) ys[t] = gf * __uint_as_float((__float_as_uint(y[4 + t]) & him) | (__float_as_uint(y[t]) & ~him));
    const v4u o = put_low(ys[0], ys[1], ys[2], ys[3], (g2 >> 1) ? thi : tlo);
    __builtin_amdgcn_raw_buffer_store_b128(o, rws, h3off, 0, AUX_SC1);
  };
  auto issue_wo = [&](v4u (&T)[8]) {
    const rsrc_t r = mk(bro ? woB : woA, NQ * DM * 2);
    const int voff = opq((orow0 + 2 * g) * (DM * 2) + w * 256 + cc * 16);
#pragma unroll
    for (int j = 0; j < 8; ++j) T[j] = ldnt(r, voff, (8 * (j & 3) + (j >> 2)) * (DM * 2));
    PIN();
  };
  auto wo_frags = [&](const v4u (&Wo)[8], v4u (&Wf)[8]) {
#pragma unroll
    for (int e = 0; e < 8; ++e) {
      const int wd = e >> 1;
      if (e & 1) Wf[e] = v4u{perm_hi(Wo[1][wd], Wo[0][wd]), perm_hi(Wo[3][wd], Wo[2][wd]), perm_hi(Wo[5][wd], Wo[4][wd]), perm_hi(Wo[7][wd], Wo[6][wd])};
      else Wf[e] = v4u{perm_lo(Wo[1][wd], Wo[0][wd]), perm_lo(Wo[3][wd], Wo[2][wd]), perm_lo(Wo[5][wd], Wo[4][wd]), perm_lo(Wo[7][wd], Wo[6][wd])};
    }
  };

  float m, l;
  f32x4 O[8];
  v4u qf[2];
  load_qf(L, 0, g, cc, qf);
  {
    const v4u P = qk_softmax<2, true>(KA, qf, zl, m, l, O);
    pv<2, true>(VA, P, O);
    dump_partial(L, 0, w, g, cc, m, l, O);
  }
  PIN();
  STAMP(7);
  load_qf(L, 1, g, cc, qf);
  {
    const v4u P = qk_softmax<4, true>(K0, qf, zl, m, l, O);
    PIN();
    issue_v<4>(V2, rvB, vvo, baseB + 2 * USTR);
    pv<4, true>(V0, P, O);
  }
  LDS_BARRIER();
  { int a0, a1, a2; unsigned a3; merge_addr(0, a0, a1, a2, a3); merge_publish(a0, a1, a2, a3); }
  PIN();
  issue_k<4>(K3, rkB, kvo, baseB + 3 * USTR);
  v4u pay = {0u, 0u, 0u, 0u};
  {
    const v4u P = qk_softmax<4, false>(K1, qf, zl, m, l, O);
    pv<4, false>(V1, P, O);
    PIN();
    issue_v<4>(V3, rvB, vvo, baseB + 3 * USTR);
    pay = __builtin_amdgcn_raw_buffer_load_b128(rws, WS_H2 + c * 8192 + tid * 16, 0, AUX_SC1);
  }
  int a_ml, a_rec, a_out; unsigned a_sel;
  merge_addr(1, a_ml, a_rec, a_out, a_sel);
  const int poff = opq(WS_H2 + c * 8192 + tid * 16);
  const int h3off = opq(WS_H3 + c * 8192 + (((g & 1) * 32 + 4 * w + (cc >> 2) + c) & 63) * 128 + ((cc & 3) * 8 + 4 * (g >> 1)) * 4);
  const int yoff = opq(WS_H3 + (tid >> 3) * 8192 + ((c + (tid >> 3)) & 63) * 128 + (tid & 7) * 16);
  STAMP(8);
  {
    const v4u P = qk_softmax<4, false>(K2, qf, zl, m, l, O);
    pv<4, false>(V2, P, O);
  }
  {
    v4u P = qk_softmax<4, false>(K3, qf, zl, m, l, O);
#pragma unroll
    for (int e = 0; e < 8; ++e) asm volatile("" : "+v"(O[e]));
    asm volatile("" : "+v"(P));
    PIN();
    STAMP(9);
    RAW_BARRIER();
    issue_wo(Wo);
    pv<4, false>(V3, P, O);
    dump_partial(L, 1, w, g, cc, m, l, O);
  }
  STAMP(10);
  LDS_BARRIER();
  merge_publish(a_ml, a_rec, a_out, a_sel);
  STAMP(11);
  if (late) pay = __builtin_amdgcn_raw_buffer_load_b128(rws, poff, 0, AUX_SC1);
  wo_frags(Wo, Wf);
#pragma unroll
  for (int e = 0; e < 8; ++e) asm volatile("" : "+v"(Wf[e]));
  consume_project(pay, Wf, poff, h3off);
  STAMP(13);
  if (c < 64) {
    const int off = yoff;
    v4u y[4];
#pragma unroll
    for (int k = 0; k < 4; ++k) y[k] = __builtin_amdgcn_raw_buffer_load_b128(rws, off, k * 64 * 8192, AUX_SC1);
    wait_lines<4, 64 * 8192, 0xFFFFFFFFu>(rws, y, off, tw);
    float v[4];
#pragma unroll
    for (int i = 0; i < 4; ++i) {
      v[i] = (hi24(y[0][i]) + hi24(y[1][i])) + (hi24(y[2][i]) + hi24(y[3][i]));
      v[i] += dpp<ROR8>(v[i]);
      v[i] = xrow_sum(v[i]);
    }
    if (lane < 8) *(LAS f32x4*)(L + L_FN + (w * 32 + 4 * lane) * 4) = f32x4{v[0], v[1], v[2], v[3]};
    STAMP(14);
    LDS_BARRIER();
    if (c == 0 && tid == 32) __hip_atomic_store((GAS u64*)(ws + WS_EPOCH), epoch + 0x9E3779B97F4A7C15ull, __ATOMIC_RELAXED, __HIP_MEMORY_SCOPE_AGENT);
    if (tid < 32) {
      float s = 0.f;
#pragma unroll
      for (int ww = 0; ww < 8; ++ww) s += *(LAS float*)(L + L_FN + (ww * 32 + tid) * 4);
      out[(c >> 5) * DM + (c & 31) * 32 + tid] = (unsigned short)(pk_bf16(s, 0.f) & 0xFFFFu);
    }
  }
}

}

extern "C" void kernel_launch(void* const* d_in, const int* in_sizes, int n_in, void* d_out, int out_size, void* d_ws, size_t ws_size,
                              hipStream_t stream) {
  (void)in_sizes; (void)n_in; (void)out_size;
  typedef const unsigned short* P;
  unsigned char* wsp = (unsigned char*)d_ws + ((ws_size - WS_TOTAL) & ~(size_t)4095);
  gated_mix_attn_decode_kernel<<<NWG, NTHR, 0, stream>>>((P)d_in[0], (P)d_in[1], (P)d_in[2], (P)d_in[3], (P)d_in[4], (P)d_in[6], (P)d_in[7],
                                                         (P)d_in[8], (P)d_in[9], (P)d_in[10], (unsigned short*)d_out, wsp);
}
```

```cpp
#include <hip/hip_runtime.h>
#include <hip/hip_bf16.h>
#include <stdint.h>

#define GAS __attribute__((address_space(1)))
#define LAS __attribute__((address_space(3)))
typedef unsigned v4u __attribute__((ext_vector_type(4)));
typedef unsigned v2u __attribute__((ext_vector_type(2)));
typedef float f32x4 __attribute__((ext_vector_type(4)));
typedef short bf16x8 __attribute__((ext_vector_type(8)));
typedef __bf16 bf16x2_t __attribute__((ext_vector_type(2)));
typedef unsigned long long u64;

namespace {

constexpr int DM = 1024, HKV = 4, SK = 32768, DH = 128, WIN = 4096, NQ = 4096;
constexpr int NWG = 256, NTHR = 512;
constexpr float C2 = 0.08838834764831845f * 1.4426950408889634f;

constexpr int WS_EPOCH = 0;
constexpr int WS_QG = 4096;
constexpr int WS_H2 = WS_QG + 2 * 2 * 2 * 1024 * 16;
constexpr int WS_H3 = WS_H2 + NWG * 8192;
constexpr int WS_END = WS_H3 + NWG * 8192;

constexpr int AUX_NT = 2, AUX_SC1 = 16;
constexpr int OOB = 0x7FFFF000;
constexpr unsigned SPIN_CAP = 1u << 17;

constexpr int PSTR = 132;
constexpr int L_PART = 0;
constexpr int L_ML = L_PART + 2 * 8 * 8 * PSTR * 4;
constexpr int L_Q = L_ML + 2 * 8 * 8 * 8;
constexpr int L_QP = L_Q + 2 * 8 * 136 * 2;
constexpr int L_GP = L_QP + 8 * 128 * 4;
constexpr int L_CB = L_GP + 64;
constexpr int L_CM = L_CB + 8 * 32 * 4;
constexpr int L_FN = L_CM + 64;
constexpr int L_END = L_FN + 8 * 32 * 4;

__device__ __forceinline__ f32x4 mfma(v4u a, v4u b, f32x4 c) {
  return __builtin_amdgcn_mfma_f32_16x16x32_bf16(__builtin_bit_cast(bf16x8, a), __builtin_bit_cast(bf16x8, b), c, 0, 0, 0);
}
__device__ __forceinline__ float dot2(unsigned a, unsigned b, float c) {
  return __builtin_amdgcn_fdot2_f32_bf16(__builtin_bit_cast(bf16x2_t, a), __builtin_bit_cast(bf16x2_t, b), c, false);
}
__device__ __forceinline__ unsigned pk_bf16(float lo, float hi) {
  bf16x2_t r; r.x = (__bf16)lo; r.y = (__bf16)hi;
  return __builtin_bit_cast(unsigned, r);
}
__device__ __forceinline__ unsigned perm_lo(unsigned hi_tok, unsigned lo_tok) { return __builtin_amdgcn_perm(hi_tok, lo_tok, 0x05040100u); }
__device__ __forceinline__ unsigned perm_hi(unsigned hi_tok, unsigned lo_tok) { return __builtin_amdgcn_perm(hi_tok, lo_tok, 0x07060302u); }
template <int CTRL> __device__ __forceinline__ float dpp(float x) {
  return __builtin_bit_cast(float, __builtin_amdgcn_mov_dpp(__builtin_bit_cast(int, x), CTRL, 0xf, 0xf, true));
}
constexpr int ROR4 = 0x124, ROR8 = 0x128, XOR1 = 0xB1, XOR2 = 0x4E, HMIR = 0x141;
__device__ __forceinline__ float swap32_add(float a, float b) {
  auto r = __builtin_amdgcn_permlane32_swap(__float_as_uint(a), __float_as_uint(b), false, false);
  return __uint_as_float(r[0]) + __uint_as_float(r[1]);
}
__device__ __forceinline__ float swap16_add(float a, float b) {
  auto r = __builtin_amdgcn_permlane16_swap(__float_as_uint(a), __float_as_uint(b), false, false);
  return __uint_as_float(r[0]) + __uint_as_float(r[1]);
}
__device__ __forceinline__ float xrow_sum(float x) { x = swap16_add(x, x); return swap32_add(x, x); }
__device__ __forceinline__ float red4(float a, float b, float c, float d) {
  a += dpp<ROR8>(a); b += dpp<ROR8>(b); c += dpp<ROR8>(c); d += dpp<ROR8>(d);
  return swap32_add(swap16_add(a, b), swap16_add(c, d));
}
__device__ __forceinline__ float ex2(float x) { return __builtin_amdgcn_exp2f(x); }

#define LDS_BARRIER() asm volatile("s_waitcnt lgkmcnt(0)\n\ts_barrier" ::: "memory")
#define RAW_BARRIER() asm volatile("s_barrier" ::: "memory")
#define PIN() __builtin_amdgcn_sched_barrier(0)

__device__ __forceinline__ int opq(int v) { asm volatile("" : "+v"(v)); return v; }

typedef __amdgpu_buffer_rsrc_t rsrc_t;
__device__ __forceinline__ rsrc_t mk(const void* p, int bytes) { return __builtin_amdgcn_make_buffer_rsrc((void*)p, 0, bytes, 0x00020000); }
__device__ __forceinline__ v4u ldnt(rsrc_t r, int voff, int soff) { return __builtin_amdgcn_raw_buffer_load_b128(r, voff, soff, AUX_NT); }

template <int NG> __device__ __forceinline__ void issue_k(v4u (&K)[2 * NG], rsrc_t r, int voff, int soff) {
  voff = opq(voff);
#pragma unroll
  for (int i = 0; i < 2 * NG; ++i) K[i] = ldnt(r, voff + ((i >> 1) & 1) * 2048 + (i & 1) * 128, soff + (i >> 2) * 4096);
  PIN();
}
template <int NG> __device__ __forceinline__ void issue_v(v4u (&V)[2 * NG], rsrc_t r, int voff, int soff) {
  voff = opq(voff);
#pragma unroll
  for (int i = 0; i < 2 * NG; ++i) V[i] = ldnt(r, voff + ((i % NG) & 1) * 2048 + (i / NG) * 256, soff + ((i % NG) >> 1) * 4096);
  PIN();
}
__device__ __forceinline__ float vmax(float a, float b) { float r; asm("v_max_f32 %0, %1, %2" : "=v"(r) : "v"(a), "v"(b)); return r; }
__device__ __forceinline__ float xrow_vmax(float x) {
  auto s = __builtin_amdgcn_permlane16_swap(__float_as_uint(x), __float_as_uint(x), false, false);
  x = vmax(__uint_as_float(s[0]), __uint_as_float(s[1]));
  auto t = __builtin_amdgcn_permlane32_swap(__float_as_uint(x), __float_as_uint(x), false, false);
  return vmax(__uint_as_float(t[0]), __uint_as_float(t[1]));
}

template <int NG, bool FIRST>
__device__ __forceinline__ v4u qk_softmax(const v4u (&K)[2 * NG], const v4u (&qf)[2], bool z, float& m, float& l, f32x4 (&O)[8]) {
  float s[NG];
#pragma unroll
  for (int i = 0; i < NG; ++i) {
    f32x4 d = mfma(K[2 * i], qf[0], f32x4{0.f, 0.f, 0.f, 0.f});
    d = mfma(K[2 * i + 1], qf[1], d);
    s[i] = (z ? d[3] : d[0]) + dpp<XOR1>(z ? d[1] : d[2]);
  }
  float mx = vmax(s[0], s[1]);
  if (NG == 4) mx = vmax(mx, vmax(s[NG - 2], s[NG - 1]));
  mx = vmax(mx, dpp<XOR1>(mx));
  mx = xrow_vmax(mx);
  const float mn = FIRST ? mx : vmax(m, mx);
  const float mc = (mn == -INFINITY) ? 0.f : mn * C2;
  float p[NG], sum = 0.f;
#pragma unroll
  for (int i = 0; i < NG; ++i) { p[i] = ex2(fmaf(s[i], C2, -mc)); sum += p[i]; }
  if (FIRST) l = sum;
  else {
    const float alpha = ex2(fmaf(m, C2, -mc));
    l = fmaf(l, alpha, sum);
#pragma unroll
    for (int e = 0; e < 8; ++e) O[e] *= alpha;
  }
  m = mn;
  asm volatile("" : "+v"(l));
  const unsigned p01 = pk_bf16(p[0], p[1]), p23 = NG == 4 ? pk_bf16(p[NG - 2], p[NG - 1]) : 0u;
  const unsigned n01 = __builtin_amdgcn_mov_dpp(p01, XOR1, 0xf, 0xf, true), n23 = NG == 4 ? __builtin_amdgcn_mov_dpp(p23, XOR1, 0xf, 0xf, true) : 0u;
  return v4u{z ? n01 : p01, z ? n23 : p23, z ? p01 : n01, z ? p23 : n23};
}
template <int NG, bool FIRST>
__device__ __forceinline__ void pv(const v4u (&V)[2 * NG], v4u P, f32x4 (&O)[8]) {
#pragma unroll
  for (int e = 0; e < 8; ++e) {
    const int wd = e >> 1;
    v4u f;
    if (e & 1) {
      f.x = perm_hi(V[1][wd], V[0][wd]); f.z = perm_hi(V[NG + 1][wd], V[NG][wd]);
      if (NG == 4) { f.y = perm_hi(V[NG - 1][wd], V[NG - 2][wd]); f.w = perm_hi(V[2 * NG - 1][wd], V[2 * NG - 2][wd]); }
      else { f.y = 0u; f.w = 0u; }
    } else {
      f.x = perm_lo(V[1][wd], V[0][wd]); f.z = perm_lo(V[NG + 1][wd], V[NG][wd]);
      if (NG == 4) { f.y = perm_lo(V[NG - 1][wd], V[NG - 2][wd]); f.w = perm_lo(V[2 * NG - 1][wd], V[2 * NG - 2][wd]); }
      else { f.y = 0u; f.w = 0u; }
    }
    O[e] = mfma(f, P, FIRST ? f32x4{0.f, 0.f, 0.f, 0.f} : O[e]);
  }
}
__device__ __forceinline__ void dump_partial(LAS unsigned char* L, int br, int w, int g, int cc, float m, float l, const f32x4 (&O)[8]) {
  l += dpp<XOR1>(l);
  l = xrow_sum(l);
  if (!(cc & 1)) {
    LAS float* rec = (LAS float*)(L + L_PART) + ((br * 8 + w) * 8 + (cc >> 1)) * PSTR + 32 * g;
#pragma unroll
    for (int e = 0; e < 8; ++e) *(LAS f32x4*)(rec + 4 * e) = O[e];
    if (g == 0) *(LAS v2u*)(L + L_ML + ((br * 8 + w) * 8 + (cc >> 1)) * 8) = v2u{__float_as_uint(m * C2), __float_as_uint(l)};
  }
}
__device__ __forceinline__ void load_qf(LAS unsigned char* L, int br, int g, int cc, v4u (&qf)[2]) {
  LAS unsigned char* q = L + L_Q + ((br * 8 + (cc >> 1)) * 136 + 16 * g + 8 * (cc & 1)) * 2;
  qf[0] = *(LAS v4u*)q; qf[1] = *(LAS v4u*)(q + 128);
}
__device__ __forceinline__ v4u put_low(float a, float b, float c, float d, unsigned lb) {
  return v4u{__builtin_amdgcn_perm(__float_as_uint(a) + 0x80u, lb, 0x07060500u), __builtin_amdgcn_perm(__float_as_uint(b) + 0x80u, lb, 0x07060501u),
             __builtin_amdgcn_perm(__float_as_uint(c) + 0x80u, lb, 0x07060502u), __builtin_amdgcn_perm(__float_as_uint(d) + 0x80u, lb, 0x07060503u)};
}
__device__ __forceinline__ unsigned get_low(const v4u& v) {
  return __builtin_amdgcn_perm(__builtin_amdgcn_perm(v.w, v.z, 0x0c0c0400u), __builtin_amdgcn_perm(v.y, v.x, 0x0c0c0400u), 0x05040100u);
}
__device__ __forceinline__ float hi24(unsigned u) { return __uint_as_float(u & ~0xFFu); }
__device__ __forceinline__ bool line_bad(unsigned diff) {
  diff |= __builtin_amdgcn_mov_dpp(diff, XOR1, 0xf, 0xf, true);
  diff |= __builtin_amdgcn_mov_dpp(diff, XOR2, 0xf, 0xf, true);
  diff |= __builtin_amdgcn_mov_dpp(diff, HMIR, 0xf, 0xf, true);
  return diff != 0u;
}
template <int N, int STR, unsigned MASK>
__device__ __forceinline__ void wait_lines(rsrc_t r, v4u (&y)[N], int off, unsigned tw) {
  for (unsigned spins = 0; spins < SPIN_CAP; ++spins) {
    bool bad[N], any = false;
#pragma unroll
    for (int k = 0; k < N; ++k) { bad[k] = line_bad((get_low(y[k]) ^ tw) & MASK); any |= bad[k]; }
    if (!__any(any)) break;
    __builtin_amdgcn_s_sleep(1);
    asm volatile("" ::: "memory");
    const int o = opq(off);
#pragma unroll
    for (int k = 0; k < N; ++k)
      if (bad[k]) y[k] = __builtin_amdgcn_raw_buffer_load_b128(r, o, k * STR, AUX_SC1);
  }
}
__device__ __forceinline__ u64 mix64(u64 z) {
  z = (z ^ (z >> 30)) * 0xBF58476D1CE4E5B9ull;
  z = (z ^ (z >> 27)) * 0x94D049BB133111EBull;
  return z ^ (z >> 31);
}

__global__ void __launch_bounds__(NTHR, 2)
gated_mix_attn_decode_kernel(const unsigned short* __restrict__ x, const unsigned short* __restrict__ kA, const unsigned short* __restrict__ vA,
                             const unsigned short* __restrict__ wqA, const unsigned short* __restrict__ woA,
                             const unsigned short* __restrict__ kB, const unsigned short* __restrict__ vB,
                             const unsigned short* __restrict__ wqB, const unsigned short* __restrict__ woB,
                             const unsigned short* __restrict__ gate_w, unsigned short* __restrict__ out, unsigned char* ws) {
  __shared__ __attribute__((aligned(16))) unsigned char smem[L_END];
  LAS unsigned char* L = (LAS unsigned char*)smem;
  const int tid = threadIdx.x, lane = tid & 63, w = __builtin_amdgcn_readfirstlane(tid >> 6), c = blockIdx.x;
  const int g = lane >> 4, cc = lane & 15;
  const int b = c >> 7, kvh = (c >> 5) & 3, sp = c & 31;
  const int col0 = kvh * 1024 + (sp & 15) * 64, kh = sp >> 4;
  const int bro = c >> 7, orow0 = (c & 127) * 32;
  const bool late = bro != 0;

  const rsrc_t rws = mk(ws, WS_END);
  const rsrc_t rwq = mk(bro ? wqB : wqA, DM * NQ * 2);
  const rsrc_t rkA = mk(kA, 2 * HKV * SK * DH * 2), rvA = mk(vA, 2 * HKV * SK * DH * 2);
  const rsrc_t rkB = mk(kB, 2 * HKV * SK * DH * 2), rvB = mk(vB, 2 * HKV * SK * DH * 2);

  const v2u epoch_v = __builtin_amdgcn_raw_buffer_load_b64(rws, opq(WS_EPOCH), 0, 0);

  auto h2slot = [&](int br, int h, int part) {
    const int cons = br * 128 + (kvh * 8 + h) * 4 + (part >> 3);
    return WS_H2 + cons * 8192 + (b * 32 + ((sp + cons) & 31)) * 128 + (part & 7) * 16;
  };
  const int rr = lane >> 3, cq = lane & 7;
  const v4u x0 = *(const GAS v4u*)(x + 512 * kh + 64 * w + 8 * rr);
  const v4u x1 = *(const GAS v4u*)(x + DM + 512 * kh + 64 * w + 8 * rr);
  const unsigned gx0 = *(const GAS unsigned*)(x + 2 * tid), gx1 = *(const GAS unsigned*)(x + DM + 2 * tid), gw = *(const GAS unsigned*)(gate_w + 2 * tid);
  const int kvo = (cc >> 1) * 256 + (2 * g + (cc & 1)) * 16;
  const int vvo = g * 2 * 256 + cc * 16;
  const bool zl = cc & 1;
  const int baseA = (((b * HKV + kvh) * SK) + (SK - WIN) + sp * 128 + w * 16) * (DH * 2);
  const int baseB = (((b * HKV + kvh) * SK) + sp * 1024 + w * 32) * (DH * 2);
  constexpr int USTR = 256 * DH * 2;
  v4u Wq[8], KA[4], VA[4], K0[8], V0[8], K1[8], V1[8];
  {
    const int voff = (512 * kh + 64 * w + 8 * rr) * (NQ * 2) + col0 * 2 + cq * 16;
#pragma unroll
    for (int j = 0; j < 8; ++j) Wq[j] = ldnt(rwq, voff, j * (NQ * 2));
  }
  PIN();
  RAW_BARRIER();
  issue_k<2>(KA, rkA, kvo, baseA);
  issue_v<2>(VA, rvA, vvo, baseA);
  {
    float a0[8], a1[8];
#pragma unroll
    for (int e = 0; e < 8; ++e) { a0[e] = 0.f; a1[e] = 0.f; }
#pragma unroll
    for (int jp = 0; jp < 4; ++jp) {
#pragma unroll
      for (int d = 0; d < 4; ++d) {
        const unsigned lo = perm_lo(Wq[2 * jp + 1][d], Wq[2 * jp][d]), hi = perm_hi(Wq[2 * jp + 1][d], Wq[2 * jp][d]);
        a0[2 * d] = dot2(lo, x0[jp], a0[2 * d]); a0[2 * d + 1] = dot2(hi, x0[jp], a0[2 * d + 1]);
        a1[2 * d] = dot2(lo, x1[jp], a1[2 * d]); a1[2 * d + 1] = dot2(hi, x1[jp], a1[2 * d + 1]);
      }
    }
    float r1[8], r2[4];
#pragma unroll
    for (int e = 0; e < 8; ++e) r1[e] = swap32_add(a0[e], a1[e]);
#pragma unroll
    for (int e = 0; e < 4; ++e) { r2[e] = swap16_add(r1[e], r1[e + 4]); r2[e] += dpp<ROR8>(r2[e]); }
    float gs = swap32_add(dot2(gw, gx0, 0.f), dot2(gw, gx1, 0.f));
    gs = swap16_add(gs, gs);
    gs += dpp<ROR8>(gs); gs += dpp<ROR4>(gs); gs += dpp<XOR2>(gs); gs += dpp<XOR1>(gs);
    if ((cc >> 3) == 0)
      *(LAS f32x4*)(L + L_QP + (w * 128 + (g >> 1) * 64 + 8 * cq + 4 * (g & 1)) * 4) = f32x4{r2[0], r2[1], r2[2], r2[3]};
    if ((lane & 31) == 0) *(LAS float*)(L + L_GP + (w * 2 + (lane >> 5)) * 4) = gs;
  }
  LDS_BARRIER();
  const u64 epoch = ((u64)(unsigned)__builtin_amdgcn_readfirstlane(epoch_v.y) << 32) | (unsigned)__builtin_amdgcn_readfirstlane(epoch_v.x);
  const u64 tag = mix64(epoch + 0x9E3779B97F4A7C15ull);
  const unsigned thi = (unsigned)(tag >> 32), tlo = (unsigned)tag;
  const unsigned tw = (lane & 1) ? thi : tlo;
  if (w == 0 && lane < 32) {
    f32x4 q = {0.f, 0.f, 0.f, 0.f};
#pragma unroll
    for (int ww = 0; ww < 8; ++ww) q += *(LAS f32x4*)(L + L_QP + (ww * 128 + lane * 4) * 4);
    __builtin_amdgcn_raw_buffer_store_b128(put_low(q[0], q[1], q[2], q[3], tw), rws, WS_QG + (((bro * 2 + (lane >> 4)) * 2 + kh) * 1024 + (col0 >> 2) + (lane & 15)) * 16, 0, AUX_SC1);
  }
  PIN();
  issue_k<4>(K0, rkB, kvo, baseB);
  issue_v<4>(V0, rvB, vvo, baseB);
  __builtin_amdgcn_s_setprio(3);
  RAW_BARRIER();
  const int qoff = WS_QG + ((((tid >> 8) * 2 + b) * 2) * 1024 + kvh * 256 + (tid & 255)) * 16;
  v4u ga = __builtin_amdgcn_raw_buffer_load_b128(rws, qoff, 0, AUX_SC1);
  v4u gb = __builtin_amdgcn_raw_buffer_load_b128(rws, qoff, 1024 * 16, AUX_SC1);
  PIN();
  __builtin_amdgcn_s_setprio(0);
  issue_k<4>(K1, rkB, kvo, baseB + 1 * USTR);
  issue_v<4>(V1, rvB, vvo, baseB + 1 * USTR);
  {
    auto stale = [&](const v4u& v) { return line_bad(get_low(v) ^ tw); };
    if (__any(stale(ga) | stale(gb))) {
      unsigned spins = 0;
      for (;;) {
        __builtin_amdgcn_s_sleep(2);
        asm volatile("" ::: "memory");
        const int qo = opq(qoff);
        const bool sa = stale(ga), sb = stale(gb);
        if (sa) ga = __builtin_amdgcn_raw_buffer_load_b128(rws, qo, 0, AUX_SC1);
        if (sb) gb = __builtin_amdgcn_raw_buffer_load_b128(rws, qo, 1024 * 16, AUX_SC1);
        if (!__any(stale(ga) | stale(gb)) || ++spins > SPIN_CAP) break;
      }
    }
    float q[4];
#pragma unroll
    for (int i = 0; i < 4; ++i) q[i] = hi24(ga[i]) + hi24(gb[i]);
    *(LAS v2u*)(L + L_Q + (((tid >> 8) * 8 + ((tid >> 5) & 7)) * 136 + 4 * (tid & 31)) * 2) = v2u{pk_bf16(q[0], q[1]), pk_bf16(q[2], q[3])};
  }
  LDS_BARRIER();

  v4u K2[8], V2[8], K3[8], V3[8], Wo[8], Wf[8];
  issue_k<4>(K2, rkB, kvo, baseB + 2 * USTR);
  float gf;
  {
    float z = 0.f;
#pragma unroll
    for (int ww = 0; ww < 8; ++ww) z += *(LAS float*)(L + L_GP + (ww * 2 + (g & 1)) * 4);
    const float gate = 1.f / (1.f + __expf(-z));
    gf = bro ? 1.f - gate : gate;
    asm volatile("" : "+v"(gf));
  }
  auto merge_publish = [&](int a_ml, int a_rec, int a_out, unsigned a_sel) {
    LAS unsigned char* ml = L + a_ml;
    LAS unsigned char* rec = L + a_rec;
    float mw[4], lw[4];
    f32x4 aw[4];
#pragma unroll
    for (int k = 0; k < 4; ++k) { const v2u t = *(LAS v2u*)(ml + k * 64); mw[k] = __uint_as_float(t.x); lw[k] = __uint_as_float(t.y); aw[k] = *(LAS f32x4*)(rec + k * 8 * PSTR * 4); }
    float M = vmax(vmax(mw[0], mw[1]), vmax(mw[2], mw[3]));
    { auto t = __builtin_amdgcn_permlane32_swap(__float_as_uint(M), __float_as_uint(M), false, false); M = vmax(__uint_as_float(t[0]), __uint_as_float(t[1])); }
    const float Ms = (M == -INFINITY) ? 0.f : M;
    float Ls = 0.f;
    f32x4 acc = {0.f, 0.f, 0.f, 0.f};
#pragma unroll
    for (int k = 0; k < 4; ++k) { const float f = ex2(mw[k] - Ms); Ls = fmaf(f, lw[k], Ls); acc += f * aw[k]; }
    Ls = swap32_add(Ls, Ls);
    const unsigned by = __builtin_amdgcn_perm(__float_as_uint(Ls), __float_as_uint(M), a_sel);
    const unsigned lb = (tw & 0xFCFCFCFCu) | ((by | (by << 6) | (by << 12) | (by << 18)) & 0x03030303u);
    const v4u o = put_low(swap32_add(acc[0], acc[0]), swap32_add(acc[1], acc[1]), swap32_add(acc[2], acc[2]), swap32_add(acc[3], acc[3]), lb);
    __builtin_amdgcn_raw_buffer_store_b128(o, rws, a_out, 0, AUX_SC1);
  };
  auto merge_addr = [&](int br, int& a_ml, int& a_rec, int& a_out, unsigned& a_sel) {
    const int ln = opq(lane), half = ln >> 5, part = ln & 31;
    a_ml = opq(L_ML + ((br * 8 + 4 * half) * 8 + w) * 8);
    a_rec = opq(L_PART + (((br * 8 + 4 * half) * 8 + w) * PSTR + 4 * part) * 4);
    a_out = opq(half ? OOB : h2slot(br, w, part));
    a_sel = (unsigned)opq(0x0C0C0C00 | (part & 7));
  };
  auto consume_project = [&](v4u pay, const v4u (&Wf)[8], int poff, int h3off) {
    v4u py[1] = {pay};
    wait_lines<1, 0, 0xFCFCFCFCu>(rws, py, poff, tw);
    pay = py[0];
    unsigned lowb = get_low(pay);
    const int ln = opq(lane);
    lowb &= 0x03030303u;
    unsigned wd = ((lowb | (lowb >> 6) | (lowb >> 12) | (lowb >> 18)) & 0xFFu) << (8 * (ln & 3));
    wd |= __builtin_amdgcn_mov_dpp(wd, XOR1, 0xf, 0xf, true);
    wd |= __builtin_amdgcn_mov_dpp(wd, XOR2, 0xf, 0xf, true);
    const unsigned ot = __builtin_amdgcn_mov_dpp(wd, HMIR, 0xf, 0xf, true);
    const float Ms = __uint_as_float((ln & 4) ? ot : wd), Lp = __uint_as_float((ln & 4) ? wd : ot);
    float M = vmax(Ms, dpp<ROR8>(Ms));
    M = xrow_vmax(M);
    const float Mz = (M == -INFINITY) ? 0.f : M;
    const float f = ex2(Ms - Mz);
    float Lw = f * Lp;
    Lw += dpp<ROR8>(Lw); Lw = xrow_sum(Lw);
    const float vs = red4(f * hi24(pay[0]), f * hi24(pay[1]), f * hi24(pay[2]), f * hi24(pay[3]));
    if ((ln & 8) == 0) *(LAS float*)(L + L_CB + (w * 32 + 4 * (ln & 7) + (ln >> 4)) * 4) = vs;
    if (ln == 8) *(LAS v2u*)(L + L_CM + w * 8) = v2u{__float_as_uint(M), __float_as_uint(Lw)};
    LDS_BARRIER();
    const int ob = (ln >> 2) & 1, g2 = ln >> 4;
    LAS float* cb = (LAS float*)(L + L_CB) + ob * 128 + 8 * g2;
    float mk_[4], lk_[4];
    f32x4 a0[4], a1[4];
#pragma unroll
    for (int k = 0; k < 4; ++k) {
      const v2u t = *(LAS v2u*)(L + L_CM + (4 * ob + k) * 8); mk_[k] = __uint_as_float(t.x); lk_[k] = __uint_as_float(t.y);
      a0[k] = *(LAS f32x4*)(cb + k * 32); a1[k] = *(LAS f32x4*)(cb + k * 32 + 4);
    }
    const float M2 = vmax(vmax(mk_[0], mk_[1]), vmax(mk_[2], mk_[3])), M2z = (M2 == -INFINITY) ? 0.f : M2;
    float Lt = 0.f;
    f32x4 o0 = {0.f, 0.f, 0.f, 0.f}, o1 = {0.f, 0.f, 0.f, 0.f};
#pragma unroll
    for (int k = 0; k < 4; ++k) { const float fk = ex2(mk_[k] - M2z); Lt = fmaf(fk, lk_[k], Lt); o0 += fk * a0[k]; o1 += fk * a1[k]; }
    const float inv = __builtin_amdgcn_rcpf(Lt);
    o0 *= inv; o1 *= inv;
    const v4u of = {pk_bf16(o0[0], o0[1]), pk_bf16(o0[2], o0[3]), pk_bf16(o1[0], o1[1]), pk_bf16(o1[2], o1[3])};
    float y[8];
#pragma unroll
    for (int e = 0; e < 8; ++e) y[e] = mfma(of, Wf[e], f32x4{0.f, 0.f, 0.f, 0.f})[0];
    const unsigned him = g2 >= 2 ? ~0u : 0u;
    float ys[4];
#pragma unroll
    for (int t = 0; t < 4; ++t) ys[t] = gf * __uint_as_float((__float_as_uint(y[4 + t]) & him) | (__float_as_uint(y[t]) & ~him));
    const v4u o = put_low(ys[0], ys[1], ys[2], ys[3], tw);
    __builtin_amdgcn_raw_buffer_store_b128(o, rws, h3off, 0, AUX_SC1);
  };
  auto issue_wo = [&](v4u (&T)[8]) {
    const rsrc_t r = mk(bro ? woB : woA, NQ * DM * 2);
    const int voff = opq((orow0 + 2 * g) * (DM * 2) + w * 256 + cc * 16);
#pragma unroll
    for (int j = 0; j < 8; ++j) T[j] = ldnt(r, voff, (8 * (j & 3) + (j >> 2)) * (DM * 2));
    PIN();
  };
  auto wo_frags = [&](const v4u (&Wo)[8], v4u (&Wf)[8]) {
#pragma unroll
    for (int e = 0; e < 8; ++e) {
      const int wd = e >> 1;
      if (e & 1) Wf[e] = v4u{perm_hi(Wo[1][wd], Wo[0][wd]), perm_hi(Wo[3][wd], Wo[2][wd]), perm_hi(Wo[5][wd], Wo[4][wd]), perm_hi(Wo[7][wd], Wo[6][wd])};
      else Wf[e] = v4u{perm_lo(Wo[1][wd], Wo[0][wd]), perm_lo(Wo[3][wd], Wo[2][wd]), perm_lo(Wo[5][wd], Wo[4][wd]), perm_lo(Wo[7][wd], Wo[6][wd])};
    }
  };

  float m, l;
  f32x4 O[8];
  v4u qf[2];
  load_qf(L, 0, g, cc, qf);
  {
    const v4u P = qk_softmax<2, true>(KA, qf, zl, m, l, O);
    pv<2, true>(VA, P, O);
    dump_partial(L, 0, w, g, cc, m, l, O);
  }
  PIN();
  load_qf(L, 1, g, cc, qf);
  {
    const v4u P = qk_softmax<4, true>(K0, qf, zl, m, l, O);
    PIN();
    issue_v<4>(V2, rvB, vvo, baseB + 2 * USTR);
    pv<4, true>(V0, P, O);
  }
  LDS_BARRIER();
  { int a0, a1, a2; unsigned a3; merge_addr(0, a0, a1, a2, a3); merge_publish(a0, a1, a2, a3); }
  PIN();
  issue_k<4>(K3, rkB, kvo, baseB + 3 * USTR);
  v4u pay = {0u, 0u, 0u, 0u};
  {
    const v4u P = qk_softmax<4, false>(K1, qf, zl, m, l, O);
    pv<4, false>(V1, P, O);
    PIN();
    issue_v<4>(V3, rvB, vvo, baseB + 3 * USTR);
    pay = __builtin_amdgcn_raw_buffer_load_b128(rws, WS_H2 + c * 8192 + tid * 16, 0, AUX_SC1);
  }
  int a_ml, a_rec, a_out; unsigned a_sel;
  merge_addr(1, a_ml, a_rec, a_out, a_sel);
  const int poff = opq(WS_H2 + c * 8192 + tid * 16);
  const int h3off = opq(WS_H3 + c * 8192 + (((((g & 1) * 8 + w) * 2 + (cc >> 3)) * 2 + (g >> 1) + c) & 63) * 128 + (lane & 7) * 16);
  const int yoff = opq(WS_H3 + (tid >> 3) * 8192 + ((c + (tid >> 3)) & 63) * 128 + (tid & 7) * 16);
  {
    const v4u P = qk_softmax<4, false>(K2, qf, zl, m, l, O);
    pv<4, false>(V2, P, O);
  }
  {
    v4u P = qk_softmax<4, false>(K3, qf, zl, m, l, O);
#pragma unroll
    for (int e = 0; e < 8; ++e) asm volatile("" : "+v"(O[e]));
    asm volatile("" : "+v"(P));
    PIN();
    RAW_BARRIER();
    issue_wo(Wo);
    pv<4, false>(V3, P, O);
    dump_partial(L, 1, w, g, cc, m, l, O);
  }
  LDS_BARRIER();
  merge_publish(a_ml, a_rec, a_out, a_sel);
  if (late) pay = __builtin_amdgcn_raw_buffer_load_b128(rws, poff, 0, AUX_SC1);
  wo_frags(Wo, Wf);
#pragma unroll
  for (int e = 0; e < 8; ++e) asm volatile("" : "+v"(Wf[e]));
  consume_project(pay, Wf, poff, h3off);
  if (c < 64) {
    const int off = yoff;
    v4u y[4];
#pragma unroll
    for (int k = 0; k < 4; ++k) y[k] = __builtin_amdgcn_raw_buffer_load_b128(rws, off, k * 64 * 8192, AUX_SC1);
    wait_lines<4, 64 * 8192, 0xFFFFFFFFu>(rws, y, off, tw);
    float v[4];
#pragma unroll
    for (int i = 0; i < 4; ++i) v[i] = (hi24(y[0][i]) + hi24(y[1][i])) + (hi24(y[2][i]) + hi24(y[3][i]));
    const float vs = red4(v[0], v[1], v[2], v[3]);
    if ((lane & 8) == 0) *(LAS float*)(L + L_FN + (w * 32 + 4 * (lane & 7) + (lane >> 4)) * 4) = vs;
    LDS_BARRIER();
    if (c == 0 && tid == 32) __hip_atomic_store((GAS u64*)(ws + WS_EPOCH), epoch + 0x9E3779B97F4A7C15ull, __ATOMIC_RELAXED, __HIP_MEMORY_SCOPE_AGENT);
    if (tid < 32) {
      float s = 0.f;
#pragma unroll
      for (int ww = 0; ww < 8; ++ww) s += *(LAS float*)(L + L_FN + (ww * 32 + tid) * 4);
      out[(c >> 5) * DM + ((c >> 2) & 7) * 128 + ((c >> 1) & 1) * 64 + (tid >> 2) * 8 + (c & 1) * 4 + (tid & 3)] = (unsigned short)(pk_bf16(s, 0.f) & 0xFFFFu);
    }
  }
}

}

extern "C" void kernel_launch(void* const* d_in, const int* in_sizes, int n_in, void* d_out, int out_size, void* d_ws, size_t ws_size,
                              hipStream_t stream) {
  (void)in_sizes; (void)n_in; (void)out_size;
  typedef const unsigned short* P;
  unsigned char* wsp = (unsigned char*)((((uintptr_t)d_ws + ws_size) - WS_END) & ~(uintptr_t)4095);
  gated_mix_attn_decode_kernel<<<NWG, NTHR, 0, stream>>>((P)d_in[0], (P)d_in[1], (P)d_in[2], (P)d_in[3], (P)d_in[4], (P)d_in[6], (P)d_in[7],
                                                         (P)d_in[8], (P)d_in[9], (P)d_in[10], (unsigned short*)d_out, wsp);
}
```

```cpp
#include <hip/hip_runtime.h>
#include <hip/hip_bf16.h>
#include <stdint.h>

#define GAS __attribute__((address_space(1)))
#define LAS __attribute__((address_space(3)))
typedef unsigned v4u __attribute__((ext_vector_type(4)));
typedef unsigned v2u __attribute__((ext_vector_type(2)));
typedef float f32x4 __attribute__((ext_vector_type(4)));
typedef short bf16x8 __attribute__((ext_vector_type(8)));
typedef __bf16 bf16x2_t __attribute__((ext_vector_type(2)));
typedef unsigned long long u64;

namespace {

constexpr int DM = 1024, HKV = 4, SK = 32768, DH = 128, WIN = 4096, NQ = 4096;
constexpr int NWG = 256, NTHR = 512;
constexpr float C2 = 0.08838834764831845f * 1.4426950408889634f;

constexpr int WS_EPOCH = 0;
constexpr int WS_QG = 4096;
constexpr int WS_H2 = WS_QG + 2 * 2 * 2 * 1024 * 16;
constexpr int WS_H3 = WS_H2 + NWG * 8192;
constexpr int WS_END = WS_H3 + NWG * 8192;

constexpr int AUX_NT = 2, AUX_SC1 = 16;
constexpr int OOB = 0x7FFFF000;
constexpr unsigned SPIN_CAP = 1u << 17;

constexpr int PSTR = 132;
constexpr int L_PART = 0;
constexpr int L_ML = L_PART + 2 * 8 * 8 * PSTR * 4;
constexpr int L_Q = L_ML + 2 * 8 * 8 * 8;
constexpr int L_QP = L_Q + 2 * 8 * 136 * 2;
constexpr int L_GP = L_QP + 8 * 128 * 4;
constexpr int L_CB = L_GP + 64;
constexpr int L_CM = L_CB + 8 * 32 * 4;
constexpr int L_FN = L_CM + 64;
constexpr int L_EP = L_FN + 8 * 32 * 4;
constexpr int L_END = L_EP + 16;

__device__ __forceinline__ f32x4 mfma(v4u a, v4u b, f32x4 c) {
  return __builtin_amdgcn_mfma_f32_16x16x32_bf16(__builtin_bit_cast(bf16x8, a), __builtin_bit_cast(bf16x8, b), c, 0, 0, 0);
}
__device__ __forceinline__ float dot2(unsigned a, unsigned b, float c) {
  return __builtin_amdgcn_fdot2_f32_bf16(__builtin_bit_cast(bf16x2_t, a), __builtin_bit_cast(bf16x2_t, b), c, false);
}
__device__ __forceinline__ unsigned pk_bf16(float lo, float hi) {
  bf16x2_t r; r.x = (__bf16)lo; r.y = (__bf16)hi;
  return __builtin_bit_cast(unsigned, r);
}
__device__ __forceinline__ unsigned perm_lo(unsigned hi_tok, unsigned lo_tok) { return __builtin_amdgcn_perm(hi_tok, lo_tok, 0x05040100u); }
__device__ __forceinline__ unsigned perm_hi(unsigned hi_tok, unsigned lo_tok) { return __builtin_amdgcn_perm(hi_tok, lo_tok, 0x07060302u); }
template <int CTRL> __device__ __forceinline__ float dpp(float x) {
  return __builtin_bit_cast(float, __builtin_amdgcn_mov_dpp(__builtin_bit_cast(int, x), CTRL, 0xf, 0xf, true));
}
constexpr int ROR4 = 0x124, ROR8 = 0x128, XOR1 = 0xB1, XOR2 = 0x4E, HMIR = 0x141;
__device__ __forceinline__ float swap32_add(float a, float b) {
  auto r = __builtin_amdgcn_permlane32_swap(__float_as_uint(a), __float_as_uint(b), false, false);
  return __uint_as_float(r[0]) + __uint_as_float(r[1]);
}
__device__ __forceinline__ float swap16_add(float a, float b) {
  auto r = __builtin_amdgcn_permlane16_swap(__float_as_uint(a), __float_as_uint(b), false, false);
  return __uint_as_float(r[0]) + __uint_as_float(r[1]);
}
__device__ __forceinline__ float xrow_sum(float x) { x = swap16_add(x, x); return swap32_add(x, x); }
__device__ __forceinline__ float red4(float a, float b, float c, float d) {
  a += dpp<ROR8>(a); b += dpp<ROR8>(b); c += dpp<ROR8>(c); d += dpp<ROR8>(d);
  return swap32_add(swap16_add(a, b), swap16_add(c, d));
}
__device__ __forceinline__ float ex2(float x) { return __builtin_amdgcn_exp2f(x); }

#define LDS_BARRIER() asm volatile("s_waitcnt lgkmcnt(0)\n\ts_barrier" ::: "memory")
#define RAW_BARRIER() asm volatile("s_barrier" ::: "memory")
#define PIN() __builtin_amdgcn_sched_barrier(0)

__device__ __forceinline__ int opq(int v) { asm volatile("" : "+v"(v)); return v; }

typedef __amdgpu_buffer_rsrc_t rsrc_t;
__device__ __forceinline__ rsrc_t mk(const void* p, int bytes) { return __builtin_amdgcn_make_buffer_rsrc((void*)p, 0, bytes, 0x00020000); }
__device__ __forceinline__ v4u ldnt(rsrc_t r, int voff, int soff) { return __builtin_amdgcn_raw_buffer_load_b128(r, voff, soff, AUX_NT); }

template <int NG> __device__ __forceinline__ void issue_k(v4u (&K)[2 * NG], rsrc_t r, int voff, int soff) {
  voff = opq(voff);
#pragma unroll
  for (int i = 0; i < 2 * NG; ++i) K[i] = ldnt(r, voff + ((i >> 1) & 1) * 2048 + (i & 1) * 128, soff + (i >> 2) * 4096);
  PIN();
}
template <int NG> __device__ __forceinline__ void issue_v(v4u (&V)[2 * NG], rsrc_t r, int voff, int soff) {
  voff = opq(voff);
#pragma unroll
  for (int i = 0; i < 2 * NG; ++i) V[i] = ldnt(r, voff + ((i % NG) & 1) * 2048 + (i / NG) * 256, soff + ((i % NG) >> 1) * 4096);
  PIN();
}
__device__ __forceinline__ float vmax(float a, float b) { float r; asm("v_max_f32 %0, %1, %2" : "=v"(r) : "v"(a), "v"(b)); return r; }
__device__ __forceinline__ float xrow_vmax(float x) {
  auto s = __builtin_amdgcn_permlane16_swap(__float_as_uint(x), __float_as_uint(x), false, false);
  x = vmax(__uint_as_float(s[0]), __uint_as_float(s[1]));
  auto t = __builtin_amdgcn_permlane32_swap(__float_as_uint(x), __float_as_uint(x), false, false);
  return vmax(__uint_as_float(t[0]), __uint_as_float(t[1]));
}

template <int NG, bool FIRST>
__device__ __forceinline__ v4u qk_softmax(const v4u (&K)[2 * NG], const v4u (&qf)[2], bool z, float& m, float& l, f32x4 (&O)[8]) {
  float s[NG];
#pragma unroll
  for (int i = 0; i < NG; ++i) {
    f32x4 d = mfma(K[2 * i], qf[0], f32x4{0.f, 0.f, 0.f, 0.f});
    d = mfma(K[2 * i + 1], qf[1], d);
    s[i] = (z ? d[3] : d[0]) + dpp<XOR1>(z ? d[1] : d[2]);
  }
  float mx = vmax(s[0], s[1]);
  if (NG == 4) mx = vmax(mx, vmax(s[NG - 2], s[NG - 1]));
  mx = vmax(mx, dpp<XOR1>(mx));
  mx = xrow_vmax(mx);
  const float mn = FIRST ? mx : vmax(m, mx);
  const float mc = (mn == -INFINITY) ? 0.f : mn * C2;
  float p[NG], sum = 0.f;
#pragma unroll
  for (int i = 0; i < NG; ++i) { p[i] = ex2(fmaf(s[i], C2, -mc)); sum += p[i]; }
  if (FIRST) l = sum;
  else {
    const float alpha = ex2(fmaf(m, C2, -mc));
    l = fmaf(l, alpha, sum);
#pragma unroll
    for (int e = 0; e < 8; ++e) O[e] *= alpha;
  }
  m = mn;
  asm volatile("" : "+v"(l));
  const unsigned p01 = pk_bf16(p[0], p[1]), p23 = NG == 4 ? pk_bf16(p[NG - 2], p[NG - 1]) : 0u;
  const unsigned n01 = __builtin_amdgcn_mov_dpp(p01, XOR1, 0xf, 0xf, true), n23 = NG == 4 ? __builtin_amdgcn_mov_dpp(p23, XOR1, 0xf, 0xf, true) : 0u;
  return v4u{z ? n01 : p01, z ? n23 : p23, z ? p01 : n01, z ? p23 : n23};
}
template <int NG, bool FIRST>
__device__ __forceinline__ void pv(const v4u (&V)[2 * NG], v4u P, f32x4 (&O)[8]) {
#pragma unroll
  for (int e = 0; e < 8; ++e) {
    const int wd = e >> 1;
    v4u f;
    if (e & 1) {
      f.x = perm_hi(V[1][wd], V[0][wd]); f.z = perm_hi(V[NG + 1][wd], V[NG][wd]);
      if (NG == 4) { f.y = perm_hi(V[NG - 1][wd], V[NG - 2][wd]); f.w = perm_hi(V[2 * NG - 1][wd], V[2 * NG - 2][wd]); }
      else { f.y = 0u; f.w = 0u; }
    } else {
      f.x = perm_lo(V[1][wd], V[0][wd]); f.z = perm_lo(V[NG + 1][wd], V[NG][wd]);
      if (NG == 4) { f.y = perm_lo(V[NG - 1][wd], V[NG - 2][wd]); f.w = perm_lo(V[2 * NG - 1][wd], V[2 * NG - 2][wd]); }
      else { f.y = 0u; f.w = 0u; }
    }
    O[e] = mfma(f, P, FIRST ? f32x4{0.f, 0.f, 0.f, 0.f} : O[e]);
  }
}
__device__ __forceinline__ void dump_partial(LAS unsigned char* L, int br, int w, int g, int cc, float m, float l, const f32x4 (&O)[8]) {
  l += dpp<XOR1>(l);
  l = xrow_sum(l);
  if (!(cc & 1)) {
    LAS float* rec = (LAS float*)(L + L_PART) + ((br * 8 + w) * 8 + (cc >> 1)) * PSTR + 32 * g;
#pragma unroll
    for (int e = 0; e < 8; ++e) *(LAS f32x4*)(rec + 4 * e) = O[e];
    if (g == 0) *(LAS v2u*)(L + L_ML + ((br * 8 + w) * 8 + (cc >> 1)) * 8) = v2u{__float_as_uint(m * C2), __float_as_uint(l)};
  }
}
__device__ __forceinline__ void load_qf(LAS unsigned char* L, int br, int g, int cc, v4u (&qf)[2]) {
  LAS unsigned char* q = L + L_Q + ((br * 8 + (cc >> 1)) * 136 + 16 * g + 8 * (cc & 1)) * 2;
  qf[0] = *(LAS v4u*)q; qf[1] = *(LAS v4u*)(q + 128);
}
__device__ __forceinline__ v4u put_low(float a, float b, float c, float d, unsigned lb) {
  return v4u{__builtin_amdgcn_perm(__float_as_uint(a) + 0x80u, lb, 0x07060500u), __builtin_amdgcn_perm(__float_as_uint(b) + 0x80u, lb, 0x07060501u),
             __builtin_amdgcn_perm(__float_as_uint(c) + 0x80u, lb, 0x07060502u), __builtin_amdgcn_perm(__float_as_uint(d) + 0x80u, lb, 0x07060503u)};
}
__device__ __forceinline__ unsigned get_low(const v4u& v) {
  return __builtin_amdgcn_perm(__builtin_amdgcn_perm(v.w, v.z, 0x0c0c0400u), __builtin_amdgcn_perm(v.y, v.x, 0x0c0c0400u), 0x05040100u);
}
__device__ __forceinline__ float hi24(unsigned u) { return __uint_as_float(u & ~0xFFu); }
__device__ __forceinline__ bool line_bad(unsigned diff) {
  diff |= __builtin_amdgcn_mov_dpp(diff, XOR1, 0xf, 0xf, true);
  diff |= __builtin_amdgcn_mov_dpp(diff, XOR2, 0xf, 0xf, true);
  diff |= __builtin_amdgcn_mov_dpp(diff, HMIR, 0xf, 0xf, true);
  return diff != 0u;
}
template <int N, int STR, unsigned MASK>
__device__ __forceinline__ void wait_lines(rsrc_t r, v4u (&y)[N], int off, unsigned tw) {
  for (unsigned spins = 0; spins < SPIN_CAP; ++spins) {
    bool bad[N], any = false;
#pragma unroll
    for (int k = 0; k < N; ++k) { bad[k] = line_bad((get_low(y[k]) ^ tw) & MASK); any |= bad[k]; }
    if (!__any(any)) break;
    __builtin_amdgcn_s_sleep(1);
    asm volatile("" ::: "memory");
    const int o = opq(off);
#pragma unroll
    for (int k = 0; k < N; ++k)
      if (bad[k]) y[k] = __builtin_amdgcn_raw_buffer_load_b128(r, o, k * STR, AUX_SC1);
  }
}
__device__ __forceinline__ u64 mix64(u64 z) {
  z = (z ^ (z >> 30)) * 0xBF58476D1CE4E5B9ull;
  z = (z ^ (z >> 27)) * 0x94D049BB133111EBull;
  return z ^ (z >> 31);
}

__global__ void __launch_bounds__(NTHR, 2)
gated_mix_attn_decode_kernel(const unsigned short* __restrict__ x, const unsigned short* __restrict__ kA, const unsigned short* __restrict__ vA,
                             const unsigned short* __restrict__ wqA, const unsigned short* __restrict__ woA,
                             const unsigned short* __restrict__ kB, const unsigned short* __restrict__ vB,
                             const unsigned short* __restrict__ wqB, const unsigned short* __restrict__ woB,
                             const unsigned short* __restrict__ gate_w, unsigned short* __restrict__ out, unsigned char* ws) {
  __shared__ __attribute__((aligned(16))) unsigned char smem[L_END];
  LAS unsigned char* L = (LAS unsigned char*)smem;
  const int tid = threadIdx.x, lane = tid & 63, w = __builtin_amdgcn_readfirstlane(tid >> 6), c = blockIdx.x;
  const int g = lane >> 4, cc = lane & 15;
  const int b = c >> 7, kvh = (c >> 5) & 3, sp = c & 31;
  const int col0 = kvh * 1024 + (sp & 15) * 64, kh = sp >> 4;
  const int bro = c >> 7, orow0 = (c & 127) * 32;
  const bool late = bro != 0;

  const rsrc_t rws = mk(ws, WS_END);
  const rsrc_t rwq = mk(bro ? wqB : wqA, DM * NQ * 2);
  const rsrc_t rkA = mk(kA, 2 * HKV * SK * DH * 2), rvA = mk(vA, 2 * HKV * SK * DH * 2);
  const rsrc_t rkB = mk(kB, 2 * HKV * SK * DH * 2), rvB = mk(vB, 2 * HKV * SK * DH * 2);

  auto h2slot = [&](int br, int h, int part) {
    const int cons = br * 128 + (kvh * 8 + h) * 4 + (part >> 3);
    return WS_H2 + cons * 8192 + (b * 32 + ((sp + cons) & 31)) * 128 + (part & 7) * 16;
  };
  const int rr = lane >> 3, cq = lane & 7;
  const int kvo = (cc >> 1) * 256 + (2 * g + (cc & 1)) * 16;
  const int vvo = g * 2 * 256 + cc * 16;
  const bool zl = cc & 1;
  const int baseA = (((b * HKV + kvh) * SK) + (SK - WIN) + sp * 128 + w * 16) * (DH * 2);
  const int baseB = (((b * HKV + kvh) * SK) + sp * 1024 + w * 32) * (DH * 2);
  constexpr int USTR = 256 * DH * 2;
  v4u Wq[8], KA[4], VA[4], K0[8], V0[8], K1[8], V1[8];
  {
    const int voff = (512 * kh + 64 * w + 8 * rr) * (NQ * 2) + col0 * 2 + cq * 16;
#pragma unroll
    for (int j = 0; j < 8; ++j) Wq[j] = ldnt(rwq, voff, j * (NQ * 2));
  }
  PIN();
  const v4u x0 = *(const GAS v4u*)(x + 512 * kh + 64 * w + 8 * rr);
  const v4u x1 = *(const GAS v4u*)(x + DM + 512 * kh + 64 * w + 8 * rr);
  const v2u epoch_v = __builtin_amdgcn_raw_buffer_load_b64(rws, opq(w == 0 ? WS_EPOCH : OOB), 0, AUX_SC1);
  PIN();
  RAW_BARRIER();
  issue_k<2>(KA, rkA, kvo, baseA);
  issue_v<2>(VA, rvA, vvo, baseA);
  const unsigned gx0 = *(const GAS unsigned*)(x + 2 * tid), gx1 = *(const GAS unsigned*)(x + DM + 2 * tid), gw = *(const GAS unsigned*)(gate_w + 2 * tid);
  PIN();
  {
    float a0[8], a1[8];
#pragma unroll
    for (int e = 0; e < 8; ++e) { a0[e] = 0.f; a1[e] = 0.f; }
#pragma unroll
    for (int jp = 0; jp < 4; ++jp) {
#pragma unroll
      for (int d = 0; d < 4; ++d) {
        const unsigned lo = perm_lo(Wq[2 * jp + 1][d], Wq[2 * jp][d]), hi = perm_hi(Wq[2 * jp + 1][d], Wq[2 * jp][d]);
        a0[2 * d] = dot2(lo, x0[jp], a0[2 * d]); a0[2 * d + 1] = dot2(hi, x0[jp], a0[2 * d + 1]);
        a1[2 * d] = dot2(lo, x1[jp], a1[2 * d]); a1[2 * d + 1] = dot2(hi, x1[jp], a1[2 * d + 1]);
      }
    }
    float r1[8], r2[4];
#pragma unroll
    for (int e = 0; e < 8; ++e) r1[e] = swap32_add(a0[e], a1[e]);
#pragma unroll
    for (int e = 0; e < 4; ++e) { r2[e] = swap16_add(r1[e], r1[e + 4]); r2[e] += dpp<ROR8>(r2[e]); }
    if ((cc >> 3) == 0)
      *(LAS f32x4*)(L + L_QP + (w * 128 + (g >> 1) * 64 + 8 * cq + 4 * (g & 1)) * 4) = f32x4{r2[0], r2[1], r2[2], r2[3]};
  }
  if (tid == 0) *(LAS v2u*)(L + L_EP) = epoch_v;
  LDS_BARRIER();
  const v2u epoch_l = *(LAS v2u*)(L + L_EP);
  const u64 epoch = ((u64)(unsigned)__builtin_amdgcn_readfirstlane(epoch_l.y) << 32) | (unsigned)__builtin_amdgcn_readfirstlane(epoch_l.x);
  const u64 tag = mix64(epoch + 0x9E3779B97F4A7C15ull);
  const unsigned thi = (unsigned)(tag >> 32), tlo = (unsigned)tag;
  const unsigned tw = (lane & 1) ? thi : tlo;
  if (w == 0 && lane < 32) {
    f32x4 q = {0.f, 0.f, 0.f, 0.f};
#pragma unroll
    for (int ww = 0; ww < 8; ++ww) q += *(LAS f32x4*)(L + L_QP + (ww * 128 + lane * 4) * 4);
    __builtin_amdgcn_raw_buffer_store_b128(put_low(q[0], q[1], q[2], q[3], tw), rws, WS_QG + (((bro * 2 + (lane >> 4)) * 2 + kh) * 1024 + (col0 >> 2) + (lane & 15)) * 16, 0, AUX_SC1);
  }
  PIN();
  issue_k<4>(K0, rkB, kvo, baseB);
  issue_v<4>(V0, rvB, vvo, baseB);
  __builtin_amdgcn_s_setprio(3);
  RAW_BARRIER();
  const int qoff = WS_QG + ((((tid >> 8) * 2 + b) * 2) * 1024 + kvh * 256 + (tid & 255)) * 16;
  v4u ga = __builtin_amdgcn_raw_buffer_load_b128(rws, qoff, 0, AUX_SC1);
  v4u gb = __builtin_amdgcn_raw_buffer_load_b128(rws, qoff, 1024 * 16, AUX_SC1);
  PIN();
  __builtin_amdgcn_s_setprio(0);
  issue_k<4>(K1, rkB, kvo, baseB + 1 * USTR);
  issue_v<4>(V1, rvB, vvo, baseB + 1 * USTR);
  {
    auto stale = [&](const v4u& v) { return line_bad(get_low(v) ^ tw); };
    if (__any(stale(ga) | stale(gb))) {
      unsigned spins = 0;
      for (;;) {
        __builtin_amdgcn_s_sleep(2);
        asm volatile("" ::: "memory");
        const int qo = opq(qoff);
        const bool sa = stale(ga), sb = stale(gb);
        if (sa) ga = __builtin_amdgcn_raw_buffer_load_b128(rws, qo, 0, AUX_SC1);
        if (sb) gb = __builtin_amdgcn_raw_buffer_load_b128(rws, qo, 1024 * 16, AUX_SC1);
        if (!__any(stale(ga) | stale(gb)) || ++spins > SPIN_CAP) break;
      }
    }
    {
      float gs = swap32_add(dot2(gw, gx0, 0.f), dot2(gw, gx1, 0.f));
      gs = swap16_add(gs, gs);
      gs += dpp<ROR8>(gs); gs += dpp<ROR4>(gs); gs += dpp<XOR2>(gs); gs += dpp<XOR1>(gs);
      if ((lane & 31) == 0) *(LAS float*)(L + L_GP + (w * 2 + (lane >> 5)) * 4) = gs;
    }
    float q[4];
#pragma unroll
    for (int i = 0; i < 4; ++i) q[i] = hi24(ga[i]) + hi24(gb[i]);
    *(LAS v2u*)(L + L_Q + (((tid >> 8) * 8 + ((tid >> 5) & 7)) * 136 + 4 * (tid & 31)) * 2) = v2u{pk_bf16(q[0], q[1]), pk_bf16(q[2], q[3])};
  }
  LDS_BARRIER();

  v4u K2[8], V2[8], K3[8], V3[8], Wo[8], Wf[8];
  issue_k<4>(K2, rkB, kvo, baseB + 2 * USTR);
  float gf;
  {
    float z = 0.f;
#pragma unroll
    for (int ww = 0; ww < 8; ++ww) z += *(LAS float*)(L + L_GP + (ww * 2 + (g & 1)) * 4);
    const float gate = 1.f / (1.f + __expf(-z));
    gf = bro ? 1.f - gate : gate;
    asm volatile("" : "+v"(gf));
  }
  auto merge_publish = [&](int a_ml, int a_rec, int a_out, unsigned a_sel) {
    LAS unsigned char* ml = L + a_ml;
    LAS unsigned char* rec = L + a_rec;
    float mw[4], lw[4];
    f32x4 aw[4];
#pragma unroll
    for (int k = 0; k < 4; ++k) { const v2u t = *(LAS v2u*)(ml + k * 64); mw[k] = __uint_as_float(t.x); lw[k] = __uint_as_float(t.y); aw[k] = *(LAS f32x4*)(rec + k * 8 * PSTR * 4); }
    float M = vmax(vmax(mw[0], mw[1]), vmax(mw[2], mw[3]));
    { auto t = __builtin_amdgcn_permlane32_swap(__float_as_uint(M), __float_as_uint(M), false, false); M = vmax(__uint_as_float(t[0]), __uint_as_float(t[1])); }
    const float Ms = (M == -INFINITY) ? 0.f : M;
    float Ls = 0.f;
    f32x4 acc = {0.f, 0.f, 0.f, 0.f};
#pragma unroll
    for (int k = 0; k < 4; ++k) { const float f = ex2(mw[k] - Ms); Ls = fmaf(f, lw[k], Ls); acc += f * aw[k]; }
    Ls = swap32_add(Ls, Ls);
    const unsigned by = __builtin_amdgcn_perm(__float_as_uint(Ls), __float_as_uint(M), a_sel);
    const unsigned lb = (tw & 0xFCFCFCFCu) | ((by | (by << 6) | (by << 12) | (by << 18)) & 0x03030303u);
    const v4u o = put_low(swap32_add(acc[0], acc[0]), swap32_add(acc[1], acc[1]), swap32_add(acc[2], acc[2]), swap32_add(acc[3], acc[3]), lb);
    __builtin_amdgcn_raw_buffer_store_b128(o, rws, a_out, 0, AUX_SC1);
  };
  auto merge_addr = [&](int br, int& a_ml, int& a_rec, int& a_out, unsigned& a_sel) {
    const int ln = opq(lane), half = ln >> 5, part = ln & 31;
    a_ml = opq(L_ML + ((br * 8 + 4 * half) * 8 + w) * 8);
    a_rec = opq(L_PART + (((br * 8 + 4 * half) * 8 + w) * PSTR + 4 * part) * 4);
    a_out = opq(half ? OOB : h2slot(br, w, part));
    a_sel = (unsigned)opq(0x0C0C0C00 | (part & 7));
  };
  auto consume_project = [&](v4u pay, const v4u (&Wf)[8], int poff, int h3off) {
    v4u py[1] = {pay};
    wait_lines<1, 0, 0xFCFCFCFCu>(rws, py, poff, tw);
    pay = py[0];
    unsigned lowb = get_low(pay);
    const int ln = opq(lane);
    lowb &= 0x03030303u;
    unsigned wd = ((lowb | (lowb >> 6) | (lowb >> 12) | (lowb >> 18)) & 0xFFu) << (8 * (ln & 3));
    wd |= __builtin_amdgcn_mov_dpp(wd, XOR1, 0xf, 0xf, true);
    wd |= __builtin_amdgcn_mov_dpp(wd, XOR2, 0xf, 0xf, true);
    const unsigned ot = __builtin_amdgcn_mov_dpp(wd, HMIR, 0xf, 0xf, true);
    const float Ms = __uint_as_float((ln & 4) ? ot : wd), Lp = __uint_as_float((ln & 4) ? wd : ot);
    float M = vmax(Ms, dpp<ROR8>(Ms));
    M = xrow_vmax(M);
    const float Mz = (M == -INFINITY) ? 0.f : M;
    const float f = ex2(Ms - Mz);
    float Lw = f * Lp;
    Lw += dpp<ROR8>(Lw); Lw = xrow_sum(Lw);
    const float vs = red4(f * hi24(pay[0]), f * hi24(pay[1]), f * hi24(pay[2]), f * hi24(pay[3]));
    if ((ln & 8) == 0) *(LAS float*)(L + L_CB + (w * 32 + 4 * (ln & 7) + (ln >> 4)) * 4) = vs;
    if (ln == 8) *(LAS v2u*)(L + L_CM + w * 8) = v2u{__float_as_uint(M), __float_as_uint(Lw)};
    LDS_BARRIER();
    const int ob = (ln >> 2) & 1, g2 = ln >> 4;
    LAS float* cb = (LAS float*)(L + L_CB) + ob * 128 + 8 * g2;
    float mk_[4], lk_[4];
    f32x4 a0[4], a1[4];
#pragma unroll
    for (int k = 0; k < 4; ++k) {
      const v2u t = *(LAS v2u*)(L + L_CM + (4 * ob + k) * 8); mk_[k] = __uint_as_float(t.x); lk_[k] = __uint_as_float(t.y);
      a0[k] = *(LAS f32x4*)(cb + k * 32); a1[k] = *(LAS f32x4*)(cb + k * 32 + 4);
    }
    const float M2 = vmax(vmax(mk_[0], mk_[1]), vmax(mk_[2], mk_[3])), M2z = (M2 == -INFINITY) ? 0.f : M2;
    float Lt = 0.f;
    f32x4 o0 = {0.f, 0.f, 0.f, 0.f}, o1 = {0.f, 0.f, 0.f, 0.f};
#pragma unroll
    for (int k = 0; k < 4; ++k) { const float fk = ex2(mk_[k] - M2z); Lt = fmaf(fk, lk_[k], Lt); o0 += fk * a0[k]; o1 += fk * a1[k]; }
    const float inv = __builtin_amdgcn_rcpf(Lt);
    o0 *= inv; o1 *= inv;
    const v4u of = {pk_bf16(o0[0], o0[1]), pk_bf16(o0[2], o0[3]), pk_bf16(o1[0], o1[1]), pk_bf16(o1[2], o1[3])};
    float y[8];
#pragma unroll
    for (int e = 0; e < 8; ++e) y[e] = mfma(of, Wf[e], f32x4{0.f, 0.f, 0.f, 0.f})[0];
    const unsigned him = g2 >= 2 ? ~0u : 0u;
    float ys[4];
#pragma unroll
    for (int t = 0; t < 4; ++t) ys[t] = gf * __uint_as_float((__float_as_uint(y[4 + t]) & him) | (__float_as_uint(y[t]) & ~him));
    const v4u o = put_low(ys[0], ys[1], ys[2], ys[3], tw);
    __builtin_amdgcn_raw_buffer_store_b128(o, rws, h3off, 0, AUX_SC1);
  };
  auto issue_wo = [&](v4u (&T)[8]) {
    const rsrc_t r = mk(bro ? woB : woA, NQ * DM * 2);
    const int voff = opq((orow0 + 2 * g) * (DM * 2) + w * 256 + cc * 16);
#pragma unroll
    for (int j = 0; j < 8; ++j) T[j] = ldnt(r, voff, (8 * (j & 3) + (j >> 2)) * (DM * 2));
    PIN();
  };
  auto wo_frags = [&](const v4u (&Wo)[8], v4u (&Wf)[8]) {
#pragma unroll
    for (int e = 0; e < 8; ++e) {
      const int wd = e >> 1;
      if (e & 1) Wf[e] = v4u{perm_hi(Wo[1][wd], Wo[0][wd]), perm_hi(Wo[3][wd], Wo[2][wd]), perm_hi(Wo[5][wd], Wo[4][wd]), perm_hi(Wo[7][wd], Wo[6][wd])};
      else Wf[e] = v4u{perm_lo(Wo[1][wd], Wo[0][wd]), perm_lo(Wo[3][wd], Wo[2][wd]), perm_lo(Wo[5][wd], Wo[4][wd]), perm_lo(Wo[7][wd], Wo[6][wd])};
    }
  };

  float m, l;
  f32x4 O[8];
  v4u qf[2];
  load_qf(L, 0, g, cc, qf);
  {
    const v4u P = qk_softmax<2, true>(KA, qf, zl, m, l, O);
    pv<2, true>(VA, P, O);
    dump_partial(L, 0, w, g, cc, m, l, O);
  }
  PIN();
  load_qf(L, 1, g, cc, qf);
  {
    const v4u P = qk_softmax<4, true>(K0, qf, zl, m, l, O);
    PIN();
    issue_v<4>(V2, rvB, vvo, baseB + 2 * USTR);
    pv<4, true>(V0, P, O);
  }
  LDS_BARRIER();
  { int a0, a1, a2; unsigned a3; merge_addr(0, a0, a1, a2, a3); merge_publish(a0, a1, a2, a3); }
  PIN();
  issue_k<4>(K3, rkB, kvo, baseB + 3 * USTR);
  v4u pay = {0u, 0u, 0u, 0u};
  {
    const v4u P = qk_softmax<4, false>(K1, qf, zl, m, l, O);
    pv<4, false>(V1, P, O);
    PIN();
    issue_v<4>(V3, rvB, vvo, baseB + 3 * USTR);
    pay = __builtin_amdgcn_raw_buffer_load_b128(rws, late ? OOB : WS_H2 + c * 8192 + tid * 16, 0, AUX_SC1);
  }
  int a_ml, a_rec, a_out; unsigned a_sel;
  merge_addr(1, a_ml, a_rec, a_out, a_sel);
  const int poff = opq(WS_H2 + c * 8192 + tid * 16);
  const int h3off = opq(WS_H3 + c * 8192 + (((((g & 1) * 8 + w) * 2 + (cc >> 3)) * 2 + (g >> 1) + c) & 63) * 128 + (lane & 7) * 16);
  const int yoff = opq(WS_H3 + (tid >> 3) * 8192 + ((c + (tid >> 3)) & 63) * 128 + (tid & 7) * 16);
  {
    const v4u P = qk_softmax<4, false>(K2, qf, zl, m, l, O);
    pv<4, false>(V2, P, O);
  }
  {
    v4u P = qk_softmax<4, false>(K3, qf, zl, m, l, O);
#pragma unroll
    for (int e = 0; e < 8; ++e) asm volatile("" : "+v"(O[e]));
    asm volatile("" : "+v"(P));
    PIN();
    RAW_BARRIER();
    issue_wo(Wo);
    pv<4, false>(V3, P, O);
    dump_partial(L, 1, w, g, cc, m, l, O);
  }
  LDS_BARRIER();
  merge_publish(a_ml, a_rec, a_out, a_sel);
  if (late) pay = __builtin_amdgcn_raw_buffer_load_b128(rws, poff, 0, AUX_SC1);
  wo_frags(Wo, Wf);
#pragma unroll
  for (int e = 0; e < 8; ++e) asm volatile("" : "+v"(Wf[e]));
  consume_project(pay, Wf, poff, h3off);
  if (c < 64) {
    const int off = yoff;
    v4u y[4];
#pragma unroll
    for (int k = 0; k < 4; ++k) y[k] = __builtin_amdgcn_raw_buffer_load_b128(rws, off, k * 64 * 8192, AUX_SC1);
    wait_lines<4, 64 * 8192, 0xFFFFFFFFu>(rws, y, off, tw);
    float v[4];
#pragma unroll
    for (int i = 0; i < 4; ++i) v[i] = (hi24(y[0][i]) + hi24(y[1][i])) + (hi24(y[2][i]) + hi24(y[3][i]));
    const float vs = red4(v[0], v[1], v[2], v[3]);
    if ((lane & 8) == 0) *(LAS float*)(L + L_FN + (w * 32 + 4 * (lane & 7) + (lane >> 4)) * 4) = vs;
    LDS_BARRIER();
    if (c == 0 && tid == 32) __hip_atomic_store((GAS u64*)(ws + WS_EPOCH), epoch + 0x9E3779B97F4A7C15ull, __ATOMIC_RELAXED, __HIP_MEMORY_SCOPE_AGENT);
    if (tid < 32) {
      float s = 0.f;
#pragma unroll
      for (int ww = 0; ww < 8; ++ww) s += *(LAS float*)(L + L_FN + (ww * 32 + tid) * 4);
      out[(c >> 5) * DM + ((c >> 2) & 7) * 128 + ((c >> 1) & 1) * 64 + (tid >> 2) * 8 + (c & 1) * 4 + (tid & 3)] = (unsigned short)(pk_bf16(s, 0.f) & 0xFFFFu);
    }
  }
}

}

extern "C" void kernel_launch(void* const* d_in, const int* in_sizes, int n_in, void* d_out, int out_size, void* d_ws, size_t ws_size,
                              hipStream_t stream) {
  (void)in_sizes; (void)n_in; (void)out_size;
  typedef const unsigned short* P;
  unsigned char* wsp = (unsigned char*)((((uintptr_t)d_ws + ws_size) - WS_END) & ~(uintptr_t)4095);
  gated_mix_attn_decode_kernel<<<NWG, NTHR, 0, stream>>>((P)d_in[0], (P)d_in[1], (P)d_in[2], (P)d_in[3], (P)d_in[4], (P)d_in[6], (P)d_in[7],
                                                         (P)d_in[8], (P)d_in[9], (P)d_in[10], (unsigned short*)d_out, wsp);
}
```
